```python
import torch

def _softmax(x, axis=-1):
    return torch.softmax(x, dim=axis)
def _gelu(x):
    return torch.nn.functional.gelu(x, approximate="tanh")
def _silu(x):
    return torch.nn.functional.silu(x)
def _sigmoid(x):
    return torch.sigmoid(x)
def _layernorm(x, axis=-1, eps=1e-5):
    m = x.mean(dim=axis, keepdim=True)
    v = x.var(dim=axis, keepdim=True, unbiased=False)
    return (x - m) / torch.sqrt(v + eps)
def _scatter_add(tgt, idx, src, axis=0):
    return tgt.clone().index_add_(axis, idx, src)
def _segment_sum(src, seg, n):
    out = torch.zeros((n,) + src.shape[1:], dtype=src.dtype)
    return out.index_add_(0, seg, src)
def _pad(x, pw):
    flat = tuple(v for pair in reversed(pw) for v in pair)
    return torch.nn.functional.pad(x, flat)

kernel_name = 'standalone__gqa_softmax__near_ridge'

def setup_inputs():

    _w = (lambda a: (lambda t: t.to(torch.bfloat16) if t.is_floating_point() else t)(torch.as_tensor(a)))
    B, Hq, Hkv, Sq, Sk, D = 32, 16, 8, 256, 256, 256
    return {
        "q": _w((lambda *s: torch.randn(*s, dtype=torch.bfloat16) * 0.8)(B, Hq, Sq, D)),
        "k": _w((lambda *s: torch.randn(*s, dtype=torch.bfloat16) * 0.8)(B, Hkv, Sk, D)),
        "v": _w((lambda *s: torch.randn(*s, dtype=torch.bfloat16) * 0.05)(B, Hkv, Sk, D)),
    }

def reference(q, k, v):
    B, Hq, Sq, D = q.shape
    Hkv = k.shape[1]
    rep = Hq // Hkv
    k = (lambda x, n, axis: torch.repeat_interleave(x, n, dim=axis))(k, rep, axis=1)
    v = (lambda x, n, axis: torch.repeat_interleave(x, n, dim=axis))(v, rep, axis=1)
    scale = D ** -0.5
    scores = torch.einsum("bhqd,bhkd->bhqk", q, k) * scale
    p = _softmax(scores, axis=-1)
    return torch.einsum("bhqk,bhkd->bhqd", p, v)
```

```cpp
#include <hip/hip_runtime.h>
#include <hip/hip_bf16.h>

typedef short bf16x8 __attribute__((ext_vector_type(8)));
typedef short s16x4 __attribute__((ext_vector_type(4)));
typedef float f32x16 __attribute__((ext_vector_type(16)));
typedef float f32x2 __attribute__((ext_vector_type(2)));
typedef __bf16 bf16x2 __attribute__((ext_vector_type(2)));
typedef unsigned u32x4 __attribute__((ext_vector_type(4)));
typedef __attribute__((address_space(3))) char* lds_ptr;
#define DI __device__ __forceinline__
#define SBAR() __builtin_amdgcn_sched_barrier(0)
#define MFMA(a, b, c) __builtin_amdgcn_mfma_f32_32x32x16_bf16(a, b, c, 0, 0, 0)
#define NT_STORE 1
#define LDPOL ""

constexpr int LDS_BYTES = 163840;
constexpr float CS = 0.0625f * 1.4426950408889634f;

DI void glds16(const char* sbase, unsigned voff, unsigned lds_dst) {
    unsigned sv;
    asm volatile("s_mov_b32 %0, m0\n\ts_mov_b32 m0, %3\n\ts_nop 0\n\tglobal_load_lds_dwordx4 %1, %2" LDPOL "\n\ts_mov_b32 m0, %0"
                 : "=&s"(sv) : "v"(voff), "s"(sbase), "s"(lds_dst) : "memory");
}
template <int OFF> DI bf16x8 gload16(const char* sbase, unsigned voff) {
    bf16x8 r;
    asm volatile("global_load_dwordx4 %0, %1, %2 offset:%3" LDPOL : "=v"(r) : "v"(voff), "s"(sbase), "i"(OFF) : "memory");
    return r;
}
DI unsigned cvtpk(float lo, float hi) { f32x2 v = {lo, hi}; bf16x2 b = __builtin_convertvector(v, bf16x2); return __builtin_bit_cast(unsigned, b); }
DI s16x4 vtr(lds_ptr p) { return __builtin_bit_cast(s16x4, __builtin_amdgcn_ds_read_tr16_b64_v4i16((__attribute__((address_space(3))) s16x4*)p)); }
DI bf16x8 ldsr(lds_ptr p) { return *(const __attribute__((address_space(3))) bf16x8*)p; }
DI int lane_id() { int l; asm volatile("v_mbcnt_lo_u32_b32 %0, -1, 0\n\tv_mbcnt_hi_u32_b32 %0, -1, %0" : "=v"(l)); return l; }
#define OPAQUE(x) asm volatile("" : "+v"(x))
#define MX3(a, b, c) __builtin_fmaxf(__builtin_fmaxf((a), (b)), (c))
#define WAITV(N) asm volatile("s_waitcnt vmcnt(" #N ")" ::: "memory")
#define BAR() asm volatile("s_waitcnt lgkmcnt(0)\n\ts_barrier" ::: "memory")

DI float softmax_pack(f32x16 (&s)[8], bf16x8 (&p)[16]) {
    float a = s[0][0], b = s[0][1];
#pragma unroll
    for (int kb = 0; kb < 8; ++kb)
#pragma unroll
        for (int r = 0; r < 16; r += 4) { a = MX3(a, s[kb][r], s[kb][r + 1]); b = MX3(b, s[kb][r + 2], s[kb][r + 3]); }
    float m = __builtin_fmaxf(a, b);
    { auto rr = __builtin_amdgcn_permlane32_swap(__float_as_uint(m), __float_as_uint(m), false, false); m = __builtin_fmaxf(__uint_as_float(rr[0]), __uint_as_float(rr[1])); }
    const float nm = -m * CS;
    float l0 = 0.f, l1 = 0.f;
#pragma unroll
    for (int kb = 0; kb < 8; ++kb) {
#pragma unroll
        for (int r = 0; r < 16; ++r) s[kb][r] = __builtin_amdgcn_exp2f(__builtin_fmaf(s[kb][r], CS, nm));
#pragma unroll
        for (int r = 0; r < 16; r += 2) { l0 += s[kb][r]; l1 += s[kb][r + 1]; }
#pragma unroll
        for (int hh = 0; hh < 2; ++hh) {
            u32x4 w = {cvtpk(s[kb][8 * hh + 0], s[kb][8 * hh + 1]), cvtpk(s[kb][8 * hh + 2], s[kb][8 * hh + 3]), cvtpk(s[kb][8 * hh + 4], s[kb][8 * hh + 5]), cvtpk(s[kb][8 * hh + 6], s[kb][8 * hh + 7])};
            p[2 * kb + hh] = __builtin_bit_cast(bf16x8, w);
        }
    }
    float l = l0 + l1;
    { auto rr = __builtin_amdgcn_permlane32_swap(__float_as_uint(l), __float_as_uint(l), false, false); l = __uint_as_float(rr[0]) + __uint_as_float(rr[1]); }
    return __builtin_amdgcn_rcpf(l);
}

__global__ __launch_bounds__(512, 1) void gqa_kernel(const char* __restrict__ Q, const char* __restrict__ K, const char* __restrict__ V, char* __restrict__ O) {
    extern __shared__ __attribute__((aligned(16))) char lds_raw[];
    const lds_ptr lds = (lds_ptr)lds_raw;
    const unsigned lds0 = (unsigned)(uintptr_t)lds_raw;
    const int tid = threadIdx.x, lane = tid & 63, r32 = lane & 31, hi = lane >> 5;
    const int wid = __builtin_amdgcn_readfirstlane(tid >> 6);
    const size_t wg = blockIdx.x;
    const char* Qa = Q + wg * 262144 + wid * 16384;
    const char* Kb = K + wg * 131072;
    const char* Vb = V + wg * 131072;
    char* Oa = O + wg * 262144 + wid * 16384;

    const unsigned qoff = r32 * 512 + hi * 16;
    const unsigned koff = (lane >> 3) * 512 + (((lane & 7) ^ ((4 * wid + (lane >> 4)) & 7)) << 4);
    const unsigned voff = (lane >> 3) * 512 + (((lane & 7) ^ (((lane >> 4) & 1) << 1)) << 4);

    bf16x8 qa[16], qb[8];
#define LQA(i) qa[i] = gload16<(i) * 32>(Qa, qoff)
    LQA(0); LQA(1); LQA(2); LQA(3); LQA(4); LQA(5); LQA(6); LQA(7); LQA(8); LQA(9); LQA(10); LQA(11); LQA(12); LQA(13); LQA(14); LQA(15);
#pragma unroll
    for (int t = 0; t < 4; ++t)
#pragma unroll
        for (int g = 0; g < 4; ++g) glds16(Kb + (64 * t + 8 * wid) * 512 + g * 128, koff, lds0 + g * 32768 + (64 * t + 8 * wid) * 128);
    const char* Qbp = Qa + 131072;
#define LQB(i) qb[(i) & 7] = gload16<(i) * 32>(Qbp, qoff)
    LQB(0); LQB(1); LQB(2); LQB(3); LQB(4); LQB(5); LQB(6); LQB(7);
#define DMA_V(s, dst) do { _Pragma("unroll") for (int i = 0; i < 4; ++i) glds16(Vb + (32 * wid + 8 * i) * 512 + (s) * 128, voff, lds0 + (dst) + (32 * wid + 8 * i) * 128); } while (0)
    DMA_V(0, 131072);

    lds_ptr kp[4], kph[4];
#pragma unroll
    for (int j = 0; j < 4; ++j) { kp[j] = lds + r32 * 128 + (((2 * j) ^ hi ^ ((r32 >> 1) & 7)) << 4); kph[j] = kp[j] + 65536; OPAQUE(kph[j]); }
#define KFRAG(kb, ks) ldsr((((ks) >> 3) ? kph[(ks) & 3] : kp[(ks) & 3]) + (((ks) >> 2) & 1) * 32768 + (kb) * 4096)

    f32x16 s[8];
    const f32x16 zero = {};
    asm volatile("s_waitcnt vmcnt(24)" : "+v"(qa[0]), "+v"(qa[1]), "+v"(qa[2]), "+v"(qa[3]), "+v"(qa[4]), "+v"(qa[5]), "+v"(qa[6]), "+v"(qa[7]) :: "memory");
    asm volatile("s_barrier" : "+v"(qa[8]), "+v"(qa[9]), "+v"(qa[10]), "+v"(qa[11]), "+v"(qa[12]), "+v"(qa[13]), "+v"(qa[14]), "+v"(qa[15]) :: "memory");
#define SA_TILE(t) do { _Pragma("unroll") for (int ks = 0; ks < 16; ++ks) { \
        s[2 * (t)] = MFMA(KFRAG(2 * (t), ks), qa[ks], ks == 0 ? zero : s[2 * (t)]); \
        s[2 * (t) + 1] = MFMA(KFRAG(2 * (t) + 1, ks), qa[ks], ks == 0 ? zero : s[2 * (t) + 1]); } } while (0)
    SA_TILE(0);
    SBAR(); asm volatile("s_waitcnt vmcnt(20)\n\ts_barrier" ::: "memory"); SBAR();
    SA_TILE(1);
    SBAR(); asm volatile("s_waitcnt vmcnt(16)\n\ts_barrier" ::: "memory"); SBAR();
    SA_TILE(2);
    SBAR(); asm volatile("s_waitcnt vmcnt(12)\n\ts_barrier" ::: "memory"); SBAR();
    SA_TILE(3);
    bf16x8 pa[16], pb[16];
    const float ila = softmax_pack(s, pa);
    SBAR();
#define SB_STEP(ks, N) do { asm volatile("s_waitcnt vmcnt(" #N ")" : "+v"(qb[(ks) & 7]) :: "memory"); \
        _Pragma("unroll") for (int kb = 0; kb < 8; ++kb) s[kb] = MFMA(KFRAG(kb, ks), qb[(ks) & 7], (ks) == 0 ? zero : s[kb]); SBAR(); } while (0)
    SB_STEP(0, 11); LQB(8);
    SB_STEP(1, 11); LQB(9);
    SB_STEP(2, 11); LQB(10);
    SB_STEP(3, 11); LQB(11);
    SB_STEP(4, 11); LQB(12);
    SB_STEP(5, 11); LQB(13);
    SB_STEP(6, 11); LQB(14);
    SB_STEP(7, 11); LQB(15);
    BAR();
#pragma unroll
    for (int j = 0; j < 4; ++j) { kph[j] = kp[j] + 65536; OPAQUE(kph[j]); }
    DMA_V(1, 0); DMA_V(2, 32768);
    SB_STEP(8, 15); SB_STEP(9, 14); SB_STEP(10, 13); SB_STEP(11, 12);
    BAR();
    DMA_V(3, 65536);
    SB_STEP(12, 15); SB_STEP(13, 14); SB_STEP(14, 13); SB_STEP(15, 12);
    BAR();
    const float ilb = softmax_pack(s, pb);
    SBAR();
    const int ln = lane_id(), r32b = ln & 31, hib = ln >> 5;
    const int g16 = (ln >> 4) & 1, q4 = (ln >> 2) & 3, p4 = ln & 3;
    lds_ptr vp0[2];
#pragma unroll
    for (int b = 0; b < 2; ++b) vp0[b] = lds + (4 * hib + q4) * 128 + ((4 * (p4 & 1) + 2 * (b ^ (q4 >> 1)) + g16) << 4) + 8 * (p4 >> 1);
    const lds_ptr stw = lds + 98304 + wid * 4096 + r32b * 128;
    const lds_ptr str = lds + 98304 + wid * 4096 + (ln >> 3) * 128 + (((ln & 7) ^ (ln >> 3)) << 4);
    const unsigned ooff = (ln >> 3) * 512 + (ln & 7) * 16;
#define OSTORE(o0, o1, il, obase) do { \
        _Pragma("unroll") for (int c = 0; c < 4; ++c) { const f32x16& o_ = (c < 2) ? o0 : o1; const int r0 = 8 * (c & 1); \
            u32x4 w = {cvtpk(o_[r0] * il, o_[r0 + 1] * il), cvtpk(o_[r0 + 2] * il, o_[r0 + 3] * il), cvtpk(o_[r0 + 4] * il, o_[r0 + 5] * il), cvtpk(o_[r0 + 6] * il, o_[r0 + 7] * il)}; \
            *(__attribute__((address_space(3))) u32x4*)(stw + (((4 * hib + c) ^ (r32b & 7)) << 4)) = w; } \
        asm volatile("s_waitcnt lgkmcnt(0)" ::: "memory"); \
        _Pragma("unroll") for (int i = 0; i < 4; ++i) { u32x4 w = *(const __attribute__((address_space(3))) u32x4*)(str + i * 1024); \
            if (NT_STORE) __builtin_nontemporal_store(w, (u32x4*)((obase) + i * 4096 + ooff)); else *(u32x4*)((obase) + i * 4096 + ooff) = w; } \
        asm volatile("s_waitcnt lgkmcnt(0)" ::: "memory"); } while (0)
#define PV_SLICE(sl, vbase) do { f32x16 oa0, oa1, ob0, ob1; lds_ptr vp[2] = {vp0[0] + (vbase), vp0[1] + (vbase)}; OPAQUE(vp[0]); OPAQUE(vp[1]); \
        _Pragma("unroll") for (int kk = 0; kk < 16; ++kk) { \
            s16x4 l0 = vtr(vp[0] + kk * 2048), h0 = vtr(vp[0] + kk * 2048 + 1024); \
            s16x4 l1 = vtr(vp[1] + kk * 2048), h1 = vtr(vp[1] + kk * 2048 + 1024); \
            bf16x8 v0 = __builtin_shufflevector(l0, h0, 0, 1, 2, 3, 4, 5, 6, 7), v1 = __builtin_shufflevector(l1, h1, 0, 1, 2, 3, 4, 5, 6, 7); \
            oa0 = MFMA(v0, pa[kk], kk == 0 ? zero : oa0); ob0 = MFMA(v0, pb[kk], kk == 0 ? zero : ob0); \
            oa1 = MFMA(v1, pa[kk], kk == 0 ? zero : oa1); ob1 = MFMA(v1, pb[kk], kk == 0 ? zero : ob1); } \
        OSTORE(oa0, oa1, ila, Oa + (sl) * 128); OSTORE(ob0, ob1, ilb, Oa + 131072 + (sl) * 128); SBAR(); } while (0)
    PV_SLICE(0, 131072);
    asm volatile("s_waitcnt vmcnt(16)\n\ts_barrier" ::: "memory"); SBAR();
    PV_SLICE(1, 0);
    asm volatile("s_waitcnt vmcnt(20)\n\ts_barrier" ::: "memory"); SBAR();
    PV_SLICE(2, 32768);
    asm volatile("s_waitcnt vmcnt(24)\n\ts_barrier" ::: "memory"); SBAR();
    PV_SLICE(3, 65536);
}

extern "C" void kernel_launch(void* const* d_in, const int* in_sizes, int n_in, void* d_out, int out_size, void* d_ws, size_t ws_size, hipStream_t stream) {
    hipFuncSetAttribute((const void*)gqa_kernel, hipFuncAttributeMaxDynamicSharedMemorySize, LDS_BYTES);
    gqa_kernel<<<256, 512, LDS_BYTES, stream>>>((const char*)d_in[0], (const char*)d_in[1], (const char*)d_in[2], (char*)d_out);
}
```

```cpp
#include <hip/hip_runtime.h>
#include <hip/hip_bf16.h>

typedef short bf16x8 __attribute__((ext_vector_type(8)));
typedef short s16x4 __attribute__((ext_vector_type(4)));
typedef float f32x16 __attribute__((ext_vector_type(16)));
typedef float f32x2 __attribute__((ext_vector_type(2)));
typedef __bf16 bf16x2 __attribute__((ext_vector_type(2)));
typedef unsigned u32x4 __attribute__((ext_vector_type(4)));
typedef __attribute__((address_space(3))) char* lds_ptr;
#define DI __device__ __forceinline__
#define SBAR() __builtin_amdgcn_sched_barrier(0)
#define MFMA(a, b, c) __builtin_amdgcn_mfma_f32_32x32x16_bf16(a, b, c, 0, 0, 0)
#define QPOL " nt"
#define KPOL " nt"
#define OPOL " sc1"
#define STAMP(i)
#if defined(__HIP_DEVICE_COMPILE__)
#define NOPK __attribute__((target("no-packed-fp32-ops")))
#else
#define NOPK
#endif

constexpr int LDS_BYTES = 163840;
constexpr float CS = 0.0625f * 1.4426950408889634f;

DI void glds16(const char* sbase, unsigned voff, unsigned lds_dst) {
    unsigned sv;
    asm volatile("s_mov_b32 %0, m0\n\ts_mov_b32 m0, %3\n\ts_nop 0\n\tglobal_load_lds_dwordx4 %1, %2" KPOL "\n\ts_mov_b32 m0, %0"
                 : "=&s"(sv) : "v"(voff), "s"(sbase), "s"(lds_dst) : "memory");
}
template <int OFF> DI bf16x8 gload16(const char* sbase, unsigned voff) {
    bf16x8 r;
    asm volatile("global_load_dwordx4 %0, %1, %2 offset:%3" QPOL : "=v"(r) : "v"(voff), "s"(sbase), "i"(OFF) : "memory");
    return r;
}
DI void gstore16(char* sbase, unsigned voff, u32x4 d) { asm volatile("global_store_dwordx4 %0, %1, %2" OPOL "\n\ts_nop 1" :: "v"(voff), "v"(d), "s"(sbase) : "memory"); }
DI void qswap(bf16x8& a, bf16x8& b) {
    u32x4 x = __builtin_bit_cast(u32x4, a), y = __builtin_bit_cast(u32x4, b);
#pragma unroll
    for (int j = 0; j < 4; ++j) { auto r = __builtin_amdgcn_permlane16_swap(x[j], y[j], false, false); x[j] = r[0]; y[j] = r[1]; }
    a = __builtin_bit_cast(bf16x8, x); b = __builtin_bit_cast(bf16x8, y);
}
DI unsigned cvtpk(float lo, float hi) { f32x2 v = {lo, hi}; bf16x2 b = __builtin_convertvector(v, bf16x2); return __builtin_bit_cast(unsigned, b); }
DI s16x4 vtr(lds_ptr p) { return __builtin_bit_cast(s16x4, __builtin_amdgcn_ds_read_tr16_b64_v4i16((__attribute__((address_space(3))) s16x4*)p)); }
DI bf16x8 ldsr(lds_ptr p) { return *(const __attribute__((address_space(3))) bf16x8*)p; }
DI int lane_id() { int l; asm volatile("v_mbcnt_lo_u32_b32 %0, -1, 0\n\tv_mbcnt_hi_u32_b32 %0, -1, %0" : "=v"(l)); return l; }
#define OPAQUE(x) asm volatile("" : "+v"(x))
#define MX3(a, b, c) __builtin_fmaxf(__builtin_fmaxf((a), (b)), (c))
#define BAR() do { SBAR(); asm volatile("s_barrier" ::: "memory"); SBAR(); } while (0)
#define WB(N) do { SBAR(); asm volatile("s_waitcnt vmcnt(" #N ")\n\ts_barrier" ::: "memory"); SBAR(); } while (0)

template <typename F> DI float softmax_pack(f32x16 (&s)[8], bf16x8 (&p)[16], F hook) {
    float a = s[0][0], b = s[0][1];
#pragma unroll
    for (int kb = 0; kb < 8; ++kb)
#pragma unroll
        for (int r = 0; r < 16; r += 4) { a = MX3(a, s[kb][r], s[kb][r + 1]); b = MX3(b, s[kb][r + 2], s[kb][r + 3]); }
    float m = __builtin_fmaxf(a, b);
    { auto rr = __builtin_amdgcn_permlane32_swap(__float_as_uint(m), __float_as_uint(m), false, false); m = __builtin_fmaxf(__uint_as_float(rr[0]), __uint_as_float(rr[1])); }
    const float nm = -m * CS;
    float l0 = 0.f, l1 = 0.f;
#pragma unroll
    for (int kb = 0; kb < 8; ++kb) {
        SBAR(); hook(kb); SBAR();
#pragma unroll
        for (int hh = 0; hh < 2; ++hh) {
            u32x4 w;
#pragma unroll
            for (int j = 0; j < 4; ++j) {
                const float x0 = __builtin_amdgcn_exp2f(__builtin_fmaf(s[kb][8 * hh + 2 * j], CS, nm)), x1 = __builtin_amdgcn_exp2f(__builtin_fmaf(s[kb][8 * hh + 2 * j + 1], CS, nm));
                l0 += x0; l1 += x1;
                w[j] = cvtpk(x0, x1);
            }
            p[2 * kb + hh] = __builtin_bit_cast(bf16x8, w); OPAQUE(p[2 * kb + hh]);
        }
    }
    float l = l0 + l1;
    { auto rr = __builtin_amdgcn_permlane32_swap(__float_as_uint(l), __float_as_uint(l), false, false); l = __uint_as_float(rr[0]) + __uint_as_float(rr[1]); }
    float il = __builtin_amdgcn_rcpf(l); OPAQUE(il); return il;
}

template <bool Y> DI void body(const char* __restrict__ Q, const char* __restrict__ K, const char* __restrict__ V, char* __restrict__ O, const int wid, char* lds_raw
) {
    STAMP(0);
    const lds_ptr lds = (lds_ptr)lds_raw;
    const unsigned lds0 = (unsigned)(uintptr_t)lds_raw;
    const int lane = lane_id(), r32 = lane & 31, hi = lane >> 5;
    const int w4 = wid & 3;
    const size_t wg = blockIdx.x;
    const char* Qa = Q + wg * 262144 + wid * 16384;
    const char* Kb = K + wg * 131072 + w4 * 4096;
    const char* Vb = V + wg * 131072 + w4 * 4096;
    char* Oa = O + wg * 262144 + wid * 16384;
    const int rot = (blockIdx.x >> 3) & 3;

    const unsigned qoff = (lane & 15) * 512 + ((lane >> 4) & 1) * 32 + hi * 16;
    const unsigned ldsw = lds0 + w4 * 1024;
    bf16x8 qa[16], qb[8];
#define LQA(i) qa[i] = gload16<((i) >> 1) * 64>(Qa + ((i) & 1) * 8192, qoff)
#define LQB(i) qb[(i) & 7] = gload16<((i) >> 1) * 64>(Qa + 131072 + ((i) & 1) * 8192, qoff)
#define LQA4(g) do { LQA(4 * (g)); LQA(4 * (g) + 2); LQA(4 * (g) + 1); LQA(4 * (g) + 3); } while (0)
#define LQB4(g) do { LQB(4 * (g)); LQB(4 * (g) + 2); LQB(4 * (g) + 1); LQB(4 * (g) + 3); } while (0)
#define LK(g, j) glds16(Kb + (j) * 16384 + (g) * 128, koff, ldsw + (g) * 32768 + (j) * 4096)
#define LK8(g) do { _Pragma("unroll") for (int j = 0; j < 8; ++j) LK(g, j); } while (0)
#define LV(s, j, dst) glds16(Vb + (j) * 16384 + (((s) + rot) & 3) * 128, voff, ldsw + (dst) + (j) * 4096)
#define VOFF() const int lv_ = lane_id(); const unsigned voff = (lv_ >> 3) * 512 + (((lv_ & 7) ^ (((lv_ >> 4) & 1) << 1)) << 4)
#define QWAIT(q, o, N) do { asm volatile("s_waitcnt vmcnt(" #N ")" : "+v"(q[o]), "+v"(q[o + 1]), "+v"(q[o + 2]), "+v"(q[o + 3]) :: "memory"); qswap(q[o], q[o + 1]); qswap(q[o + 2], q[o + 3]); SBAR(); } while (0)

    if (!Y) { LQA4(0); LQA4(1); LQA4(2); LQA4(3); }
    else {
        const unsigned koff = (lane >> 3) * 512 + (((lane & 7) ^ ((4 * w4 + (lane >> 4)) & 7)) << 4);
        LK8(0); LK8(1); WB(8); LK8(2); WB(8); LK8(3); WB(8); LQA4(0); LQA4(1); WB(8);
    }
    lds_ptr kp[4];
#pragma unroll
    for (int j = 0; j < 4; ++j) kp[j] = lds + r32 * 128 + (((2 * j) ^ hi ^ ((r32 >> 1) & 7)) << 4);
    f32x16 s[8];
    const f32x16 zero = {};
    bf16x8 kf[4];
#define KFR(i) ldsr(kp[((i) >> 3) & 3] + (((i) >> 5) & 1) * 32768 + ((i) & 7) * 4096)
#define KADV(d) do { _Pragma("unroll") for (int j = 0; j < 4; ++j) { kp[j] += (d); OPAQUE(kp[j]); } } while (0)
#define KPRE(g) do { if ((g) == 2) KADV(65536); kf[0] = KFR(32 * (g)); kf[1] = KFR(32 * (g) + 1); kf[2] = KFR(32 * (g) + 2); SBAR(); } while (0)
#define SGRP(g, q, qo, CONT, HOOK) do { \
        _Pragma("unroll") for (int ii = 0; ii < 32; ++ii) { const int i = 32 * (g) + ii; \
            if (CONT && i + 3 == 64) KADV(65536); \
            if (ii + 3 < 32 || (CONT && i + 3 < 128)) kf[(i + 3) & 3] = KFR(i + 3); \
            s[i & 7] = MFMA(kf[i & 3], q[(qo) + (ii >> 3)], i < 8 ? zero : s[i & 7]); \
            HOOK; SBAR(); } } while (0)
    if (!Y) {
        QWAIT(qa, 0, 12); QWAIT(qa, 4, 8); QWAIT(qa, 8, 4); QWAIT(qa, 12, 0);
        BAR(); STAMP(1); KPRE(0); SGRP(0, qa, 0, false, );
        BAR(); KPRE(1); SGRP(1, qa, 4, false, );
        BAR(); KPRE(2); SGRP(2, qa, 8, false, );
        BAR(); KPRE(3); SGRP(3, qa, 12, false, );
    } else {
        __builtin_amdgcn_s_setprio(1);
        KPRE(0);
        QWAIT(qa, 0, 4); STAMP(1); SGRP(0, qa, 0, true, if (ii == 1) { LQA(8); LQA(10); } if (ii == 9) { LQA(9); LQA(11); } if (ii == 17) { LQA(12); LQA(14); } if (ii == 25) { LQA(13); LQA(15); });
        QWAIT(qa, 4, 8);  SGRP(1, qa, 4, true, );
        QWAIT(qa, 8, 4);  SGRP(2, qa, 8, true, );
        QWAIT(qa, 12, 0); SGRP(3, qa, 12, true, if (ii == 3) { LQB(0); LQB(2); } if (ii == 11) { LQB(1); LQB(3); } if (ii == 19) { LQB(4); LQB(6); } if (ii == 27) { LQB(5); LQB(7); });
        __builtin_amdgcn_s_setprio(0);
    }
    STAMP(2);
    bf16x8 pa[16], pb[16];
    float ila, ilb;
    if (!Y) ila = softmax_pack(s, pa, [&](int kb) { if (kb == 3) { LQB(0); LQB(2); } if (kb == 4) { LQB(1); LQB(3); } if (kb == 5) { LQB(4); LQB(6); } if (kb == 6) { LQB(5); LQB(7); } });
    else { VOFF(); ila = softmax_pack(s, pa, [&](int kb) { LV(0, kb, 131072); }); }
    SBAR();
    STAMP(3);
    KADV(-65536); KPRE(0);
    if (!Y) {
        QWAIT(qb, 0, 4); SGRP(0, qb, 0, true, ); LQB4(2);
        QWAIT(qb, 4, 4); SGRP(1, qb, 4, true, ); LQB4(3);
        QWAIT(qb, 0, 4); SGRP(2, qb, 0, true, );
        QWAIT(qb, 4, 0); SGRP(3, qb, 4, true, );
        STAMP(4);
        BAR();
        VOFF();
#pragma unroll
        for (int j = 0; j < 8; ++j) LV(1, j, 0);
        ilb = softmax_pack(s, pb, [&](int kb) { LV(2, kb, 32768); });
        STAMP(5);
        WB(8);
    } else {
        QWAIT(qb, 0, 12); SGRP(0, qb, 0, true, ); LQB4(2);
        QWAIT(qb, 4, 12); SGRP(1, qb, 4, true, ); LQB4(3);
        BAR();
        QWAIT(qb, 0, 4); SGRP(2, qb, 0, true, );
        QWAIT(qb, 4, 0); SGRP(3, qb, 4, true, );
        STAMP(4);
        BAR();
        VOFF();
        ilb = softmax_pack(s, pb, [&](int kb) { if (kb < 4) { LV(3, 2 * kb, 65536); LV(3, 2 * kb + 1, 65536); } });
        STAMP(5);
        WB(0);
    }
    STAMP(6);
    const int ln = lane_id(), r32b = ln & 31, hib = ln >> 5;
    const int g16 = (ln >> 4) & 1, q4 = (ln >> 2) & 3, p4 = ln & 3;
    lds_ptr vp[2];
#pragma unroll
    for (int b = 0; b < 2; ++b) { vp[b] = lds + 131072 + (4 * hib + q4) * 128 + ((4 * (p4 & 1) + 2 * (b ^ (q4 >> 1)) + g16) << 4) + 8 * (p4 >> 1); OPAQUE(vp[b]); }
    lds_ptr stw = lds + 98304 + wid * 4096 + r32b * 128 + (((4 * hib) ^ (r32b & 4)) << 4);
    const int sx = (r32b & 3) << 4;
    lds_ptr str = lds + 98304 + wid * 4096 + (ln >> 3) * 128 + (((ln & 7) ^ (ln >> 3)) << 4);
    OPAQUE(stw); OPAQUE(str);
    const unsigned ooff = (ln >> 3) * 512 + (ln & 7) * 16;
    s16x4 vl[3][2], vh[3][2];
    f32x16 oc[2][2];
    u32x4 w_[4];
#define VRD(n, b) do { const int kk_ = (n) & 15; \
        if (kk_ == 0 && (n) > 0 && (((n) >> 4) & 1) == 0) { const int d_ = ((n) == 32) ? -131072 : 32768; vp[b] += d_; OPAQUE(vp[b]); } \
        vl[(n) % 3][b] = vtr(vp[b] + kk_ * 2048); vh[(n) % 3][b] = vtr(vp[b] + kk_ * 2048 + 1024); } while (0)
#define EPI(u, e) do { \
        if ((e) >= 2 && (e) < 18) { const int t_ = (e) - 2, c_ = t_ >> 2; const f32x16& o_ = oc[(u) & 1][c_ >> 1]; const int r0 = 8 * (c_ & 1) + 2 * (t_ & 3); const float il = ((u) & 1) ? ilb : ila; \
            wq[t_ & 3] = cvtpk(o_[r0] * il, o_[r0 + 1] * il); OPAQUE(wq); \
            if ((t_ & 3) == 3) *(__attribute__((address_space(3))) u32x4*)(stw + ((c_ << 4) ^ sx)) = wq; } \
        if ((e) >= 20 && (e) < 24) w_[(e) - 20] = *(const __attribute__((address_space(3))) u32x4*)(str + ((e) - 20) * 1024); \
        if ((e) >= 26 && (e) < 30) gstore16(Oa + ((u) & 1) * 131072 + ((((u) >> 1) + rot) & 3) * 128 + ((e) - 26) * 4096, ooff, w_[(e) - 26]); } while (0)
#define PVU(u, P, MID) do { \
        _Pragma("unroll") for (int e = 0; e < 32; ++e) { const int kk = e >> 1, b_ = e & 1, n = 16 * (u) + kk; \
            if (e == 31) { MID; } \
            const bf16x8 v_ = __builtin_shufflevector(vl[n % 3][b_], vh[n % 3][b_], 0, 1, 2, 3, 4, 5, 6, 7); \
            oc[(u) & 1][b_] = MFMA(v_, P[kk], kk == 0 ? zero : oc[(u) & 1][b_]); \
            if (n + 2 < 128) VRD(n + 2, b_); \
            if ((u) > 0) EPI((u) - 1, e); \
            SBAR(); } } while (0)
    u32x4 wq = {};
    VRD(0, 0); VRD(0, 1); VRD(1, 0); VRD(1, 1); SBAR();
    PVU(0, pa, ); PVU(1, pb, );
    STAMP(7);
    PVU(2, pa, if (!Y) WB(8));
    PVU(3, pb, );
    STAMP(8);
    PVU(4, pa, ); PVU(5, pb, );
    STAMP(9);
    PVU(6, pa, ); PVU(7, pb, );
    STAMP(10);
#pragma unroll
    for (int e = 0; e < 32; ++e) EPI(7, e);
    STAMP(11);
}

__global__ __launch_bounds__(512, 1) NOPK void gqa_kernel(const char* __restrict__ Q, const char* __restrict__ K, const char* __restrict__ V, char* __restrict__ O
#define DBG
) {
    extern __shared__ __attribute__((aligned(16))) char lds_raw[];
    const int wid = __builtin_amdgcn_readfirstlane(threadIdx.x >> 6);
    if (wid < 4) body<false>(Q, K, V, O, wid, lds_raw DBG); else body<true>(Q, K, V, O, wid, lds_raw DBG);
}

extern "C" void kernel_launch(void* const* d_in, const int* in_sizes, int n_in, void* d_out, int out_size, void* d_ws, size_t ws_size, hipStream_t stream) {
    (void)hipFuncSetAttribute((const void*)gqa_kernel, hipFuncAttributeMaxDynamicSharedMemorySize, LDS_BYTES);
    gqa_kernel<<<256, 512, LDS_BYTES, stream>>>((const char*)d_in[0], (const char*)d_in[1], (const char*)d_in[2], (char*)d_out
   );
}
```

```cpp
#include <hip/hip_runtime.h>
#include <hip/hip_bf16.h>

typedef short bf16x8 __attribute__((ext_vector_type(8)));
typedef short s16x4 __attribute__((ext_vector_type(4)));
typedef float f32x16 __attribute__((ext_vector_type(16)));
typedef float f32x2 __attribute__((ext_vector_type(2)));
typedef __bf16 bf16x2 __attribute__((ext_vector_type(2)));
typedef unsigned u32x4 __attribute__((ext_vector_type(4)));
typedef __attribute__((address_space(3))) char* lds_ptr;
#define DI __device__ __forceinline__
#define SBAR() __builtin_amdgcn_sched_barrier(0)
#define MFMA(a, b, c) __builtin_amdgcn_mfma_f32_32x32x16_bf16(a, b, c, 0, 0, 0)
#define QPOL " nt"
#define KPOL " nt"
#define OPOL " sc1"
#define STAMP(i)
#if defined(__HIP_DEVICE_COMPILE__)
#define NOPK __attribute__((target("no-packed-fp32-ops")))
#else
#define NOPK
#endif

constexpr int LDS_BYTES = 163840;
constexpr float CS = 0.0625f * 1.4426950408889634f;

DI void glds16(const char* sbase, unsigned voff, unsigned lds_dst) {
    unsigned sv;
    asm volatile("s_mov_b32 %0, m0\n\ts_mov_b32 m0, %3\n\ts_nop 0\n\tglobal_load_lds_dwordx4 %1, %2" KPOL "\n\ts_mov_b32 m0, %0"
                 : "=&s"(sv) : "v"(voff), "s"(sbase), "s"(lds_dst) : "memory");
}
template <int OFF> DI bf16x8 gload16(const char* sbase, unsigned voff) {
    bf16x8 r;
    asm volatile("global_load_dwordx4 %0, %1, %2 offset:%3" QPOL : "=v"(r) : "v"(voff), "s"(sbase), "i"(OFF) : "memory");
    return r;
}
DI void gstore16(char* sbase, unsigned voff, u32x4 d) { asm volatile("global_store_dwordx4 %0, %1, %2" OPOL "\n\ts_nop 1" :: "v"(voff), "v"(d), "s"(sbase) : "memory"); }
DI void qswap(bf16x8& a, bf16x8& b) {
    u32x4 x = __builtin_bit_cast(u32x4, a), y = __builtin_bit_cast(u32x4, b);
#pragma unroll
    for (int j = 0; j < 4; ++j) { auto r = __builtin_amdgcn_permlane16_swap(x[j], y[j], false, false); x[j] = r[0]; y[j] = r[1]; }
    a = __builtin_bit_cast(bf16x8, x); b = __builtin_bit_cast(bf16x8, y);
}
DI unsigned cvtpk(float lo, float hi) { f32x2 v = {lo, hi}; bf16x2 b = __builtin_convertvector(v, bf16x2); return __builtin_bit_cast(unsigned, b); }
DI s16x4 vtr(lds_ptr p) { return __builtin_bit_cast(s16x4, __builtin_amdgcn_ds_read_tr16_b64_v4i16((__attribute__((address_space(3))) s16x4*)p)); }
DI bf16x8 ldsr(lds_ptr p) { return *(const __attribute__((address_space(3))) bf16x8*)p; }
DI int lane_id() { int l; asm volatile("v_mbcnt_lo_u32_b32 %0, -1, 0\n\tv_mbcnt_hi_u32_b32 %0, -1, %0" : "=v"(l)); return l; }
#define OPAQUE(x) asm volatile("" : "+v"(x))
#define MX3(a, b, c) __builtin_fmaxf(__builtin_fmaxf((a), (b)), (c))
#define BAR() do { SBAR(); asm volatile("s_barrier" ::: "memory"); SBAR(); } while (0)
#define WB(N) do { SBAR(); asm volatile("s_waitcnt vmcnt(" #N ")\n\ts_barrier" ::: "memory"); SBAR(); } while (0)

template <typename F> DI float softmax_pack(f32x16 (&s)[8], bf16x8 (&p)[16], F hook) {
    float a = s[0][0], b = s[0][1];
#pragma unroll
    for (int kb = 0; kb < 8; ++kb)
#pragma unroll
        for (int r = 0; r < 16; r += 4) { a = MX3(a, s[kb][r], s[kb][r + 1]); b = MX3(b, s[kb][r + 2], s[kb][r + 3]); }
    float m = __builtin_fmaxf(a, b);
    { auto rr = __builtin_amdgcn_permlane32_swap(__float_as_uint(m), __float_as_uint(m), false, false); m = __builtin_fmaxf(__uint_as_float(rr[0]), __uint_as_float(rr[1])); }
    const float nm = -m * CS;
    float l0 = 0.f, l1 = 0.f;
#pragma unroll
    for (int kb = 0; kb < 8; ++kb) {
        SBAR(); hook(kb); SBAR();
#pragma unroll
        for (int hh = 0; hh < 2; ++hh) {
            u32x4 w;
#pragma unroll
            for (int j = 0; j < 4; ++j) {
                const float x0 = __builtin_amdgcn_exp2f(__builtin_fmaf(s[kb][8 * hh + 2 * j], CS, nm)), x1 = __builtin_amdgcn_exp2f(__builtin_fmaf(s[kb][8 * hh + 2 * j + 1], CS, nm));
                l0 += x0; l1 += x1;
                w[j] = cvtpk(x0, x1);
            }
            p[2 * kb + hh] = __builtin_bit_cast(bf16x8, w); OPAQUE(p[2 * kb + hh]);
        }
    }
    float l = l0 + l1;
    { auto rr = __builtin_amdgcn_permlane32_swap(__float_as_uint(l), __float_as_uint(l), false, false); l = __uint_as_float(rr[0]) + __uint_as_float(rr[1]); }
    float il = __builtin_amdgcn_rcpf(l); OPAQUE(il); return il;
}

template <bool Y> DI void body(const char* __restrict__ Q, const char* __restrict__ K, const char* __restrict__ V, char* __restrict__ O, const int wid, char* lds_raw
) {
    STAMP(0);
    const lds_ptr lds = (lds_ptr)lds_raw;
    const unsigned lds0 = (unsigned)(uintptr_t)lds_raw;
    const int lane = lane_id(), r32 = lane & 31, hi = lane >> 5;
    const int w4 = wid & 3;
    const size_t wg = blockIdx.x;
    const char* Qa = Q + wg * 262144 + wid * 16384;
    const char* Kb = K + wg * 131072 + w4 * 4096;
    const char* Vb = V + wg * 131072 + w4 * 4096;
    char* Oa = O + wg * 262144 + wid * 16384;
    const int rot = (blockIdx.x >> 3) & 3;

    const unsigned qoff = (lane & 15) * 512 + ((lane >> 4) & 1) * 32 + hi * 16;
    const unsigned ldsw = lds0 + w4 * 1024;
    bf16x8 qa[16], qb[8];
#define LQA(i) qa[i] = gload16<((i) >> 1) * 64>(Qa + ((i) & 1) * 8192, qoff)
#define LQB(i) qb[(i) & 7] = gload16<((i) >> 1) * 64>(Qa + 131072 + ((i) & 1) * 8192, qoff)
#define LQA4(g) do { LQA(4 * (g)); LQA(4 * (g) + 2); LQA(4 * (g) + 1); LQA(4 * (g) + 3); } while (0)
#define LQB4(g) do { LQB(4 * (g)); LQB(4 * (g) + 2); LQB(4 * (g) + 1); LQB(4 * (g) + 3); } while (0)
#define LK(g, j) glds16(Kb + (j) * 16384 + (g) * 128, koff, ldsw + (g) * 32768 + (j) * 4096)
#define LK8(g) do { _Pragma("unroll") for (int j = 0; j < 8; ++j) LK(g, j); } while (0)
#define LV(s, j, dst) glds16(Vb + (j) * 16384 + (((s) + rot) & 3) * 128, voff, ldsw + (dst) + (j) * 4096)
#define VOFF() const int lv_ = lane_id(); const unsigned voff = (lv_ >> 3) * 512 + (((lv_ & 7) ^ (((lv_ >> 4) & 1) << 1)) << 4)
#define QWAIT(q, o, N) do { asm volatile("s_waitcnt vmcnt(" #N ")" : "+v"(q[o]), "+v"(q[o + 1]), "+v"(q[o + 2]), "+v"(q[o + 3]) :: "memory"); qswap(q[o], q[o + 1]); qswap(q[o + 2], q[o + 3]); SBAR(); } while (0)

    if (!Y) { LQA4(0); LQA4(1); LQA4(2); LQA4(3); }
    else {
        const unsigned koff = (lane >> 3) * 512 + (((lane & 7) ^ ((4 * w4 + (lane >> 4)) & 7)) << 4);
        LK8(0); LK8(1); WB(8); LK8(2); WB(8); LK8(3); WB(8); LQA4(0); LQA4(1); WB(8);
    }
    lds_ptr kp[4];
#pragma unroll
    for (int j = 0; j < 4; ++j) kp[j] = lds + r32 * 128 + (((2 * j) ^ hi ^ ((r32 >> 1) & 7)) << 4);
    f32x16 s[8];
    const f32x16 zero = {};
    bf16x8 kf[4];
#define KFR(i) ldsr(kp[((i) >> 3) & 3] + (((i) >> 5) & 1) * 32768 + ((i) & 7) * 4096)
#define KADV(d) do { _Pragma("unroll") for (int j = 0; j < 4; ++j) { kp[j] += (d); OPAQUE(kp[j]); } } while (0)
#define KPRE(g) do { if ((g) == 2) KADV(65536); kf[0] = KFR(32 * (g)); kf[1] = KFR(32 * (g) + 1); kf[2] = KFR(32 * (g) + 2); SBAR(); } while (0)
#define SGRP(g, q, qo, CONT, HOOK) do { \
        _Pragma("unroll") for (int ii = 0; ii < 32; ++ii) { const int i = 32 * (g) + ii; \
            if (CONT && i + 3 == 64) KADV(65536); \
            if (ii + 3 < 32 || (CONT && i + 3 < 128)) kf[(i + 3) & 3] = KFR(i + 3); \
            s[i & 7] = MFMA(kf[i & 3], q[(qo) + (ii >> 3)], i < 8 ? zero : s[i & 7]); \
            HOOK; SBAR(); } } while (0)
    if (!Y) {
        QWAIT(qa, 0, 12); QWAIT(qa, 4, 8); QWAIT(qa, 8, 4); QWAIT(qa, 12, 0);
        BAR(); STAMP(1); KPRE(0); SGRP(0, qa, 0, false, );
        BAR(); KPRE(1); SGRP(1, qa, 4, false, );
        BAR(); KPRE(2); SGRP(2, qa, 8, false, );
        BAR(); KPRE(3); SGRP(3, qa, 12, false, );
    } else {
        __builtin_amdgcn_s_setprio(1);
        KPRE(0);
        QWAIT(qa, 0, 4); STAMP(1); SGRP(0, qa, 0, true, if (ii == 1) { LQA(8); LQA(10); } if (ii == 9) { LQA(9); LQA(11); } if (ii == 17) { LQA(12); LQA(14); } if (ii == 25) { LQA(13); LQA(15); });
        QWAIT(qa, 4, 8);  SGRP(1, qa, 4, true, );
        QWAIT(qa, 8, 4);  SGRP(2, qa, 8, true, );
        QWAIT(qa, 12, 0); SGRP(3, qa, 12, true, if (ii == 3) { LQB(0); LQB(2); } if (ii == 11) { LQB(1); LQB(3); } if (ii == 19) { LQB(4); LQB(6); } if (ii == 27) { LQB(5); LQB(7); });
        __builtin_amdgcn_s_setprio(0);
    }
    STAMP(2);
    bf16x8 pa[16], pb[16];
    float ila, ilb;
    if (!Y) ila = softmax_pack(s, pa, [&](int kb) { if (kb == 3) { LQB(0); LQB(2); } if (kb == 4) { LQB(1); LQB(3); } if (kb == 5) { LQB(4); LQB(6); } if (kb == 6) { LQB(5); LQB(7); } });
    else { VOFF(); ila = softmax_pack(s, pa, [&](int kb) { LV(0, kb, 131072); }); }
    SBAR();
    STAMP(3);
    KADV(-65536); KPRE(0);
    if (!Y) {
        QWAIT(qb, 0, 4); SGRP(0, qb, 0, true, ); LQB4(2);
        QWAIT(qb, 4, 4); SGRP(1, qb, 4, true, ); LQB4(3);
        QWAIT(qb, 0, 4); SGRP(2, qb, 0, true, );
        QWAIT(qb, 4, 0); SGRP(3, qb, 4, true, );
        STAMP(4);
        BAR();
        VOFF();
#pragma unroll
        for (int j = 0; j < 8; ++j) LV(1, j, 0);
        ilb = softmax_pack(s, pb, [&](int kb) { LV(2, kb, 32768); });
        STAMP(5);
        WB(8);
    } else {
        QWAIT(qb, 0, 12); SGRP(0, qb, 0, true, ); LQB4(2);
        QWAIT(qb, 4, 12); SGRP(1, qb, 4, true, ); LQB4(3);
        BAR();
        QWAIT(qb, 0, 4); SGRP(2, qb, 0, true, );
        QWAIT(qb, 4, 0); SGRP(3, qb, 4, true, );
        STAMP(4);
        BAR();
        VOFF();
        ilb = softmax_pack(s, pb, [&](int kb) { if (kb < 4) { LV(3, 2 * kb, 65536); LV(3, 2 * kb + 1, 65536); } });
        STAMP(5);
        WB(0);
    }
    STAMP(6);
    const int ln = lane_id(), r32b = ln & 31, hib = ln >> 5;
    const int g16 = (ln >> 4) & 1, q4 = (ln >> 2) & 3, p4 = ln & 3;
    lds_ptr vp[2];
#pragma unroll
    for (int b = 0; b < 2; ++b) { vp[b] = lds + 131072 + (4 * hib + q4) * 128 + ((4 * (p4 & 1) + 2 * (b ^ (q4 >> 1)) + g16) << 4) + 8 * (p4 >> 1); OPAQUE(vp[b]); }
    lds_ptr stw = lds + 98304 + wid * 4096 + r32b * 128 + (((4 * hib) ^ (r32b & 4)) << 4);
    const int sx = (r32b & 3) << 4;
    lds_ptr str = lds + 98304 + wid * 4096 + (ln >> 3) * 128 + (((ln & 7) ^ (ln >> 3)) << 4);
    OPAQUE(stw); OPAQUE(str);
    const unsigned ooff = (ln >> 3) * 512 + (ln & 7) * 16;
    s16x4 vl[3][2], vh[3][2];
    f32x16 oc[2][2];
    u32x4 w_[4];
#define VRD(n, b) do { const int kk_ = (n) & 15; \
        if (kk_ == 0 && (n) > 0 && (((n) >> 4) & 1) == 0) { const int d_ = ((n) == 32) ? -131072 : 32768; vp[b] += d_; OPAQUE(vp[b]); } \
        vl[(n) % 3][b] = vtr(vp[b] + kk_ * 2048); vh[(n) % 3][b] = vtr(vp[b] + kk_ * 2048 + 1024); } while (0)
#define EPI(u, e) do { \
        if ((e) >= 2 && (e) < 18) { const int t_ = (e) - 2, c_ = t_ >> 2; const f32x16& o_ = oc[(u) & 1][c_ >> 1]; const int r0 = 8 * (c_ & 1) + 2 * (t_ & 3); const float il = ((u) & 1) ? ilb : ila; \
            wq[t_ & 3] = cvtpk(o_[r0] * il, o_[r0 + 1] * il); OPAQUE(wq); \
            if ((t_ & 3) == 3) *(__attribute__((address_space(3))) u32x4*)(stw + ((c_ << 4) ^ sx)) = wq; } \
        if ((e) >= 20 && (e) < 24) w_[(e) - 20] = *(const __attribute__((address_space(3))) u32x4*)(str + ((e) - 20) * 1024); \
        if ((e) >= 26 && (e) < 30) gstore16(Oa + ((u) & 1) * 131072 + ((((u) >> 1) + rot) & 3) * 128 + ((e) - 26) * 4096, ooff, w_[(e) - 26]); } while (0)
#define PVU(u, P, MID) do { \
        _Pragma("unroll") for (int e = 0; e < 32; ++e) { const int kk = e >> 1, b_ = e & 1, n = 16 * (u) + kk; \
            if (e == 31) { MID; } \
            const bf16x8 v_ = __builtin_shufflevector(vl[n % 3][b_], vh[n % 3][b_], 0, 1, 2, 3, 4, 5, 6, 7); \
            oc[(u) & 1][b_] = MFMA(v_, P[kk], kk == 0 ? zero : oc[(u) & 1][b_]); \
            if (n + 2 < 128) VRD(n + 2, b_); \
            if ((u) > 0) EPI((u) - 1, e); \
            SBAR(); } } while (0)
    u32x4 wq = {};
    VRD(0, 0); VRD(0, 1); VRD(1, 0); VRD(1, 1); SBAR();
    PVU(0, pa, ); PVU(1, pb, );
    STAMP(7);
    PVU(2, pa, if (!Y) WB(8));
    PVU(3, pb, );
    STAMP(8);
    PVU(4, pa, ); PVU(5, pb, );
    STAMP(9);
    PVU(6, pa, ); PVU(7, pb, );
    STAMP(10);
#pragma unroll
    for (int e = 0; e < 32; ++e) EPI(7, e);
    STAMP(11);
}

__global__ __launch_bounds__(512, 1) NOPK void gqa_kernel(const char* __restrict__ Q, const char* __restrict__ K, const char* __restrict__ V, char* __restrict__ O
#define DBG
) {
    extern __shared__ __attribute__((aligned(16))) char lds_raw[];
    const int wid = __builtin_amdgcn_readfirstlane(threadIdx.x >> 6);
    {
        const char* pc; asm volatile("s_getpc_b64 %0" : "=s"(pc)); unsigned sv;
        asm volatile("s_mov_b32 %0, m0\n\ts_mov_b32 m0, %3\n\ts_nop 0\n\tglobal_load_lds_dword %1, %2\n\ts_mov_b32 m0, %0"
                     : "=&s"(sv) : "v"(min(threadIdx.x * 128u, 54000u)), "s"(pc), "s"((unsigned)(uintptr_t)lds_raw + 131072 + wid * 256) : "memory"); }
    if (wid < 4) body<false>(Q, K, V, O, wid, lds_raw DBG); else body<true>(Q, K, V, O, wid, lds_raw DBG);
}

extern "C" void kernel_launch(void* const* d_in, const int* in_sizes, int n_in, void* d_out, int out_size, void* d_ws, size_t ws_size, hipStream_t stream) {
    (void)hipFuncSetAttribute((const void*)gqa_kernel, hipFuncAttributeMaxDynamicSharedMemorySize, LDS_BYTES);
    gqa_kernel<<<256, 512, LDS_BYTES, stream>>>((const char*)d_in[0], (const char*)d_in[1], (const char*)d_in[2], (char*)d_out
   );
}
```

```cpp
#include <hip/hip_runtime.h>
#include <hip/hip_bf16.h>

typedef short bf16x8 __attribute__((ext_vector_type(8)));
typedef short s16x4 __attribute__((ext_vector_type(4)));
typedef unsigned short u16x8 __attribute__((ext_vector_type(8)));
typedef float f32x16 __attribute__((ext_vector_type(16)));
typedef float f32x4 __attribute__((ext_vector_type(4)));
typedef float f32x2 __attribute__((ext_vector_type(2)));
typedef __bf16 bf16x2 __attribute__((ext_vector_type(2)));
typedef unsigned u32x4 __attribute__((ext_vector_type(4)));
typedef unsigned u32x2 __attribute__((ext_vector_type(2)));
typedef __attribute__((address_space(3))) char* lds_ptr;
#define DI __device__ __forceinline__
#define SBAR() __builtin_amdgcn_sched_barrier(0)
#define MFMA32(a, b, c) __builtin_amdgcn_mfma_f32_32x32x16_bf16(a, b, c, 0, 0, 0)
#define MFMA16(a, b, c) __builtin_amdgcn_mfma_f32_16x16x32_bf16(a, b, c, 0, 0, 0)
#define OPAQUE(x) asm volatile("" : "+v"(x))
#define MX3(a, b, c) __builtin_fmaxf(__builtin_fmaxf((a), (b)), (c))
#define BAR() do { SBAR(); asm volatile("s_barrier" ::: "memory"); SBAR(); } while (0)
#define WB(N) do { SBAR(); asm volatile("s_waitcnt vmcnt(" #N ")\n\ts_barrier" ::: "memory"); SBAR(); } while (0)
#define PAD(n) do { if ((n) > 0) asm volatile("s_nop %0" :: "i"((n) - 1)); } while (0)
#define PRIO(n) __builtin_amdgcn_s_setprio(n)
#define STAMP(i)
#define STARG
#define STPASS
#if defined(__HIP_DEVICE_COMPILE__)
#define NOPK __attribute__((target("no-packed-fp32-ops")))
#else
#define NOPK
#endif
#define CODE_BYTES 41700u
#define YPV 1

constexpr int LDS_BYTES = 163840;
constexpr float CS = 0.0625f * 1.4426950408889634f;
constexpr int PA1 = 10, PY0 = 16, PY1 = 8, PA2 = 8, PA3 = 7, PY2 = 16, PB1 = 5, PB2 = 6;
constexpr int VR = 5;

DI void glds16(const char* sbase, unsigned voff, unsigned lds_dst) {
    unsigned sv;
    asm volatile("s_mov_b32 %0, m0\n\ts_mov_b32 m0, %3\n\ts_nop 0\n\tglobal_load_lds_dwordx4 %1, %2 nt\n\ts_mov_b32 m0, %0"
                 : "=&s"(sv) : "v"(voff), "s"(sbase), "s"(lds_dst) : "memory");
}
template <int OFF> DI bf16x8 gload16(const char* sbase, unsigned voff) {
    bf16x8 r;
    asm volatile("global_load_dwordx4 %0, %1, %2 offset:%3 nt" : "=v"(r) : "v"(voff), "s"(sbase), "i"(OFF) : "memory");
    return r;
}
DI void gstore16(char* sbase, unsigned voff, u32x4 d) { asm volatile("global_store_dwordx4 %0, %1, %2 sc1\n\ts_nop 1" :: "v"(voff), "v"(d), "s"(sbase) : "memory"); }
DI void swap16(bf16x8& a, bf16x8& b) {
    u32x4 x = __builtin_bit_cast(u32x4, a), y = __builtin_bit_cast(u32x4, b);
#pragma unroll
    for (int j = 0; j < 4; ++j) { auto r = __builtin_amdgcn_permlane16_swap(x[j], y[j], false, false); x[j] = r[0]; y[j] = r[1]; }
    a = __builtin_bit_cast(bf16x8, x); b = __builtin_bit_cast(bf16x8, y);
}
DI void swap8(bf16x8& a, bf16x8& b) {
    u32x4 x = __builtin_bit_cast(u32x4, a), y = __builtin_bit_cast(u32x4, b);
#pragma unroll
    for (int j = 0; j < 4; ++j) { const int xj = x[j], yj = y[j];
        x[j] = __builtin_amdgcn_update_dpp(xj, yj, 0x128, 0xf, 0xc, false); y[j] = __builtin_amdgcn_update_dpp(yj, xj, 0x128, 0xf, 0x3, false); }
    a = __builtin_bit_cast(bf16x8, x); b = __builtin_bit_cast(bf16x8, y);
}
DI unsigned cvtpk(float lo, float hi) { f32x2 v = {lo, hi}; bf16x2 b = __builtin_convertvector(v, bf16x2); return __builtin_bit_cast(unsigned, b); }
DI s16x4 vtr(unsigned p) { return __builtin_bit_cast(s16x4, __builtin_amdgcn_ds_read_tr16_b64_v4i16((__attribute__((address_space(3))) s16x4*)(uintptr_t)p)); }
DI bf16x8 ldsr(lds_ptr p) { return *(const __attribute__((address_space(3))) bf16x8*)p; }
DI int lane_id() { int l; asm volatile("v_mbcnt_lo_u32_b32 %0, -1, 0\n\tv_mbcnt_hi_u32_b32 %0, -1, %0" : "=v"(l)); return l; }

DI float row_ref(const f32x16 (&s)[4]) {
    float a = s[0][0], b = s[0][1];
#pragma unroll
    for (int kb = 0; kb < 4; ++kb)
#pragma unroll
        for (int r = 0; r < 16; r += 4) { a = MX3(a, s[kb][r], s[kb][r + 1]); b = MX3(b, s[kb][r + 2], s[kb][r + 3]); }
    const unsigned m = __float_as_uint(__builtin_fmaxf(a, b));
    auto rr = __builtin_amdgcn_permlane32_swap(m, m, false, false);
    return __builtin_ceilf(__builtin_fmaxf(__uint_as_float(rr[0]), __uint_as_float(rr[1])) * CS);
}
template <int P0, typename F> DI void exp_pack(const f32x16 (&s)[4], bf16x8 (&p)[16], float ref, F hook) {
    const float nm = -ref;
#pragma unroll
    for (int t = 0; t < 8; ++t) {
        SBAR(); hook(t); SBAR();
        u32x4 w;
#pragma unroll
        for (int j = 0; j < 4; ++j)
            w[j] = cvtpk(__builtin_amdgcn_exp2f(__builtin_fmaf(s[t >> 1][8 * (t & 1) + 2 * j], CS, nm)), __builtin_amdgcn_exp2f(__builtin_fmaf(s[t >> 1][8 * (t & 1) + 2 * j + 1], CS, nm)));
        p[P0 + t] = __builtin_bit_cast(bf16x8, w); OPAQUE(p[P0 + t]);
    }
}
DI void fixup(bf16x8 (&p)[16], float r1, float r2) {
    const unsigned di = (unsigned)__builtin_fminf(r2 - r1, 255.f) * 0x00800080u; const u32x4 d4 = {di, di, di, di};
#pragma unroll
    for (int i = 0; i < 8; ++i) { p[i] = __builtin_bit_cast(bf16x8, __builtin_elementwise_sub_sat(__builtin_bit_cast(u16x8, p[i]), __builtin_bit_cast(u16x8, d4))); OPAQUE(p[i]); }
#pragma unroll
    for (int i = 0; i < 8; ++i) { swap16(p[2 * i], p[2 * i + 1]); OPAQUE(p[2 * i]); OPAQUE(p[2 * i + 1]); }
    SBAR();
}

template <bool Y> DI void scores(const char* Qa, const char* Kw, const char* Vw, const int w4, const int rot, char* lds_raw, bf16x8 (&pa)[16], bf16x8 (&pb)[16] STARG) {
    const lds_ptr lds = (lds_ptr)lds_raw;
    const unsigned ldsw = (unsigned)(uintptr_t)lds_raw + w4 * 1024;
    const int lane = lane_id(), r32 = lane & 31, hi = lane >> 5;
    const unsigned qoff = (lane & 7) * 512 + hi * 16 + ((lane >> 3) & 3) * 32;
    const unsigned koff = (lane >> 3) * 512 + (((lane & 7) ^ ((4 * w4 + (lane >> 4)) & 7)) << 4);
    bf16x8 qa[16], qb[16], kf[4];
    f32x16 s[4];
    const f32x16 zero = {};
#define LQA(i) qa[i] = gload16<((i) >> 2) * 128>(Qa + ((i) & 3) * 4096, qoff)
#define LQA4(g) do { LQA(4 * (g)); LQA(4 * (g) + 1); LQA(4 * (g) + 2); LQA(4 * (g) + 3); } while (0)
#define QWAITA(o, N) do { asm volatile("s_waitcnt vmcnt(" #N ")" : "+v"(qa[o]), "+v"(qa[o + 1]), "+v"(qa[o + 2]), "+v"(qa[o + 3]) :: "memory"); \
        swap16(qa[o], qa[o + 2]); swap16(qa[o + 1], qa[o + 3]); swap8(qa[o], qa[o + 1]); swap8(qa[o + 2], qa[o + 3]); SBAR(); } while (0)
#define QBI(k) (((k) & ~3) | (((k) & 1) << 1) | (((k) >> 1) & 1))
#define LQB(k) qb[QBI(k)] = gload16<(QBI(k) >> 1) * 64>(Qa + 131072 + (QBI(k) & 1) * 8192, qoffb)
#define LQB2(k) do { LQB(k); LQB((k) + 1); } while (0)
#define QWAITB(o, N) do { asm volatile("s_waitcnt vmcnt(" #N ")" : "+v"(qb[o]), "+v"(qb[o + 1]), "+v"(qb[o + 2]), "+v"(qb[o + 3]) :: "memory"); swap16(qb[o], qb[o + 1]); swap16(qb[o + 2], qb[o + 3]); SBAR(); } while (0)
#define LK(h, g, j) glds16(Kw + (h) * 65536 + (j) * 16384 + (g) * 128, koff, ldsw + (h) * 65536 + (g) * 16384 + (j) * 4096)
#define LK4(h, g) do { LK(h, g, 0); LK(h, g, 1); LK(h, g, 2); LK(h, g, 3); } while (0)
#define LV(sl, j, dst) glds16(Vw + (j) * 16384 + (((sl) + rot) & 3) * 128, voff, ldsw + (dst) + (j) * 4096)
#define VOFF() const int lv_ = lane_id(); const unsigned voff = (lv_ >> 3) * 512 + (((lv_ & 7) ^ (((lv_ >> 4) & 1) << 1) ^ ((w4 >> 1) << 2)) << 4)
    if (!Y) { LQA4(0); LQA4(1); } else { LK4(0, 0); LK4(0, 1); }
    lds_ptr kp[4];
#pragma unroll
    for (int j = 0; j < 4; ++j) kp[j] = lds + r32 * 128 + (((2 * j) ^ hi ^ ((r32 >> 1) & 7)) << 4);
#define KADV(d) do { _Pragma("unroll") for (int j = 0; j < 4; ++j) { kp[j] += (d); OPAQUE(kp[j]); } } while (0)
#define KF(i) kf[(i) & 3] = ldsr(kp[((i) >> 2) & 3] + ((i) >> 4) * 16384 + ((i) & 3) * 4096)
#define S_PRE(g) do { KF(16 * (g)); KF(16 * (g) + 1); KF(16 * (g) + 2); SBAR(); } while (0)
#define S_GRP(q, g, CONT, HOOK) do { \
        _Pragma("unroll") for (int ii = 0; ii < 16; ++ii) { const int i = 16 * (g) + ii; \
            if (ii + 3 < 16 || (CONT && i + 3 < 64)) KF(i + 3); \
            s[i & 3] = MFMA32(kf[i & 3], q[i >> 2], i < 4 ? zero : s[i & 3]); \
            HOOK; SBAR(); } } while (0)
    if (!Y) {
        QWAITA(0, 4);  BAR(); STAMP(1); S_PRE(0); S_GRP(qa, 0, false, PAD(PA1); if (ii == 0) LQA(8); if (ii == 4) LQA(9); if (ii == 8) LQA(10); if (ii == 12) LQA(11));
        QWAITA(4, 4);  BAR(); S_PRE(1); S_GRP(qa, 1, false, PAD(PA1); if (ii == 0) LQA(12); if (ii == 4) LQA(13); if (ii == 8) LQA(14); if (ii == 12) LQA(15));
        QWAITA(8, 4);  BAR(); S_PRE(2); S_GRP(qa, 2, false, PAD(PA1));
        QWAITA(12, 0); BAR(); S_PRE(3); S_GRP(qa, 3, true, PAD(PA1));
    } else {
        WB(4); LK4(0, 2); LK(0, 3, 0); LK(0, 3, 1); WB(6); LK(0, 3, 2); LK(0, 3, 3); LQA4(0); WB(8); LQA4(1); LQA4(2); WB(12);
        PRIO(1); S_PRE(0);
        QWAITA(0, 8); STAMP(1); S_GRP(qa, 0, true, PAD(PY0); if (ii == 0) LQA(12); if (ii == 3) LQA(13); if (ii == 6) LQA(14); if (ii == 9) LQA(15));
        PRIO(0);
        QWAITA(4, 8); S_GRP(qa, 1, true, PAD(PY1)); QWAITA(8, 4); S_GRP(qa, 2, true, PAD(PY1)); QWAITA(12, 0); S_GRP(qa, 3, true, PAD(PY1));
        BAR();
    }
    STAMP(2);
    const int lb_ = lane_id(); const unsigned qoffb = (lb_ & 15) * 512 + ((lb_ >> 4) & 1) * 32 + (lb_ >> 5) * 16;
    float r1 = row_ref(s);
    if (!Y) { exp_pack<0>(s, pa, r1, [&](int t) { LK(1, t >> 2, t & 3); }); LK4(1, 2); LK4(1, 3); LQB2(0); WB(10); }
    else    exp_pack<0>(s, pa, r1, [&](int t) { if (t == 4) BAR(); });
    OPAQUE(r1);
    KADV(65536); S_PRE(0);
    if (!Y) {
        S_GRP(qa, 0, true, PAD(PA2); if (ii == 8) LQB2(2)); S_GRP(qa, 1, false, PAD(PA2); if (ii == 8) LQB2(4));
        WB(6); S_PRE(2);
        S_GRP(qa, 2, true, PAD(PA3); if (ii == 8) LQB2(6)); S_GRP(qa, 3, true, PAD(PA3); if (ii == 8) LQB2(8));
    } else {
        S_GRP(qa, 0, true, PAD(PY2)); S_GRP(qa, 1, true, ); S_GRP(qa, 2, true, ); S_GRP(qa, 3, true, );
        LQB2(0);
    }
    {   const float r2 = __builtin_fmaxf(r1, row_ref(s));
        if (!Y) exp_pack<8>(s, pa, r2, [&](int t) { if (t == 0) LQB2(10); if (t == 3) LQB2(12); if (t == 6) LQB2(14); });
        else    exp_pack<8>(s, pa, r2, [&](int t) { if (t == 0) LQB2(2); if (t == 2) LQB2(4); if (t == 4) LQB2(6); if (t == 5) LQB2(8); if (t == 7) LQB2(10); });
        fixup(pa, r1, r2); }
    STAMP(3);
    KADV(-65536); S_PRE(0);
    if (!Y) { QWAITB(0, 12); S_GRP(qb, 0, true, PAD(PB1)); }
    else { PRIO(1); QWAITB(0, 8); S_GRP(qb, 0, true, PAD(PB1); if (ii == 2) LQB2(12); if (ii == 10) LQB2(14)); }
    QWAITB(4, 8); S_GRP(qb, 1, true, PAD(PB1)); QWAITB(8, 4); S_GRP(qb, 2, true, PAD(PB1)); QWAITB(12, 0); S_GRP(qb, 3, true, PAD(PB1));
    if (Y) PRIO(0);
    STAMP(4);
    r1 = row_ref(s);
    if (!Y) { VOFF(); exp_pack<0>(s, pb, r1, [&](int t) { LV(0, t, 131072); }); }
    else exp_pack<0>(s, pb, r1, [&](int t) { if (t == 3) BAR(); });
    OPAQUE(r1);
    KADV(65536); S_PRE(0);
    if (Y) PRIO(1);
    S_GRP(qb, 0, true, PAD(Y ? PB2 : 0)); S_GRP(qb, 1, true, PAD(Y ? PB2 : 0)); S_GRP(qb, 2, true, ); S_GRP(qb, 3, true, );
    if (Y) PRIO(0);
    STAMP(5);
    BAR();
    {   VOFF();
        if (!Y) { _Pragma("unroll") for (int j = 0; j < 8; ++j) LV(1, j, 0); }
        const float r2 = __builtin_fmaxf(r1, row_ref(s));
        if (!Y) exp_pack<8>(s, pb, r2, [&](int t) { LV(2, t, 32768); });
        else    exp_pack<8>(s, pb, r2, [&](int t) { LV(3, t, 65536); });
        fixup(pb, r1, r2); }
    if (!Y) WB(8); else PRIO(YPV);
    STAMP(6);
}

__global__ __launch_bounds__(512, 1) NOPK void gqa_kernel(const char* __restrict__ Q, const char* __restrict__ K, const char* __restrict__ V, char* __restrict__ O
) {
    STAMP(0);
    extern __shared__ __attribute__((aligned(16))) char lds_raw[];
    const unsigned lds0 = (unsigned)(uintptr_t)lds_raw;
    const int wid = __builtin_amdgcn_readfirstlane(threadIdx.x >> 6), w4 = wid & 3;
    {
        const char* pc; asm volatile("s_getpc_b64 %0" : "=s"(pc)); unsigned sv;
        asm volatile("s_mov_b32 %0, m0\n\ts_mov_b32 m0, %3\n\ts_nop 0\n\tglobal_load_lds_dword %1, %2\n\ts_mov_b32 m0, %0"
                     : "=&s"(sv) : "v"(min(threadIdx.x * 128u, CODE_BYTES)), "s"(pc), "s"(lds0 + 131072 + wid * 256) : "memory"); }
    const size_t wg = blockIdx.x;
    const int rot = (blockIdx.x >> 3) & 3;
    const char* Qa = Q + wg * 262144 + wid * 16384;
    char* Oa = O + wg * 262144 + wid * 16384;
    asm volatile("" : "+s"(Oa));
    bf16x8 pa[16], pb[16];
    if (wid < 4) scores<false>(Qa, K + wg * 131072 + w4 * 4096, V + wg * 131072 + w4 * 4096, w4, rot, lds_raw, pa, pb STPASS);
    else         scores<true>(Qa, K + wg * 131072 + w4 * 4096, V + wg * 131072 + w4 * 4096, w4, rot, lds_raw, pa, pb STPASS);

    const int ln = lane_id(), g4 = ln >> 4, q4 = (ln >> 2) & 3, p4 = ln & 3;
    unsigned vp[2];
#pragma unroll
    for (int b = 0; b < 2; ++b) { vp[b] = lds0 + 131072 + (16 * (g4 & 1) + 4 * (g4 >> 1) + q4) * 128 + ((b ^ (q4 >> 1) ^ ((g4 & 1) << 1)) << 5) + 8 * p4; OPAQUE(vp[b]); }
    lds_ptr stw = (lds_ptr)lds_raw + 98304 + wid * 4096 + (ln & 15) * 128 + (((g4 >> 1) ^ (ln & 1)) << 4) + 8 * (g4 & 1);
    const int sx = ((ln >> 1) & 3) << 5;
    lds_ptr str = (lds_ptr)lds_raw + 98304 + wid * 4096 + (ln >> 3) * 128 + (((ln & 7) ^ (ln >> 3)) << 4);
    OPAQUE(stw); OPAQUE(str);
    const unsigned ooff = (ln >> 3) * 512 + (ln & 7) * 16;
    s16x4 vl[VR], vh[VR];
    f32x4 oc[2][2][2][2], rs[2][2];
    const f32x4 zero4 = {};
    u32x4 w_[4], hold[2] = {}; u32x2 wq = {};
    float il[2][2], m0_ = 0.f, m1_ = 0.f;
    bf16x8 ones = {0x3F80, 0x3F80, 0x3F80, 0x3F80, 0x3F80, 0x3F80, 0x3F80, 0x3F80}; OPAQUE(ones);
#define VMOVE(n) do { if (((n) & 15) < 2 && (n) >= 16) { vp[(n) & 1] ^= 64; if ((((n) >> 4) & 1) == 0) vp[(n) & 1] += ((n) < 64) ? -131072 : 32768; OPAQUE(vp[(n) & 1]); } } while (0)
#define VRL(n) do { VMOVE(n); vl[(n) % VR] = vtr(vp[(n) & 1] + (((n) >> 1) & 7) * 4096); } while (0)
#define VRH(n) vh[(n) % VR] = vtr(vp[(n) & 1] + (((n) >> 1) & 7) * 4096 + 1024)
#define STW(c2, qb, x0, x1) *(__attribute__((address_space(3))) u32x2*)(stw + (((c2) << 5) ^ sx) + 2048 * (qb)) = u32x2{x0, x1}
#define STR(i) w_[i] = *(const __attribute__((address_space(3))) u32x4*)(str + (i) * 1024)
#define OST(u, hd, i) gstore16(Oa + (hd) * 131072 + ((((u) >> 1) + rot) & 3) * 128 + (i) * 4096, ooff, w_[i])
#define OEL(u, t, k) (oc[(u) & 1][(t) >> 3][((t) >> 2) & 1][((t) >> 1) & 1][2 * ((t) & 1) + (k)] * il[(t) >> 3][((t) >> 2) & 1])
#define EPI(u, x) do { const int pb_ = (u) & 1; \
        if ((x) >= 3 && (x) < 51) { const int t_ = ((x) - 3) / 3, k_ = ((x) - 3) % 3; \
            if (k_ == 0) { m0_ = OEL(u, t_, 0); OPAQUE(m0_); } if (k_ == 1) { m1_ = OEL(u, t_, 1); OPAQUE(m1_); } \
            if (k_ == 2) { const unsigned d_ = cvtpk(m0_, m1_); \
                if (t_ < 8 || pb_ == 1) { wq[t_ & 1] = d_; OPAQUE(wq); if (t_ & 1) STW(2 * pb_ + ((t_ >> 1) & 1), (t_ >> 2) & 1, wq[0], wq[1]); } \
                else { hold[(t_ >> 2) & 1][t_ & 3] = d_; OPAQUE(hold[(t_ >> 2) & 1]); } } } \
        if (pb_ == 1) { \
            if ((x) == 27) STR(0); if ((x) == 28) STR(1); if ((x) == 30) STR(2); if ((x) == 31) STR(3); \
            if ((x) == 33) STW(0, 0, hold[0][0], hold[0][1]); if ((x) == 36) STW(1, 0, hold[0][2], hold[0][3]); if ((x) == 39) STW(0, 1, hold[1][0], hold[1][1]); if ((x) == 42) STW(1, 1, hold[1][2], hold[1][3]); \
            if ((x) == 40) OST(u, 0, 0); if ((x) == 46) OST(u, 0, 1); if ((x) == 52) OST(u, 0, 2); if ((x) == 56) OST(u, 0, 3); \
            if ((x) == 54) STR(0); if ((x) == 58) STR(1); if ((x) == 60) STR(2); if ((x) == 62) STR(3); } } while (0)
#define EPB(u, x) do { if ((x) == 1) OST(u, 1, 0); if ((x) == 16) OST(u, 1, 1); if ((x) == 34) OST(u, 1, 2); if ((x) == 54) OST(u, 1, 3); } while (0)
#define PVH(u, X0, E, MID) do { \
        _Pragma("unroll") for (int x = (X0); x < (X0) + 16; ++x) { const int qb_ = x & 1, hd = (x >> 1) & 1, b_ = (x >> 2) & 1, m_ = x >> 3, n = 16 * (u) + (x >> 2); \
            if (x == (E)) { MID; } \
            const bf16x8 v_ = __builtin_shufflevector(vl[n % VR], vh[n % VR], 0, 1, 2, 3, 4, 5, 6, 7); \
            oc[(u) & 1][hd][qb_][b_] = MFMA16(v_, hd ? pb[2 * m_ + qb_] : pa[2 * m_ + qb_], m_ == 0 ? zero4 : oc[(u) & 1][hd][qb_][b_]); \
            if ((u) == 0 && b_ == 0) { SBAR(); rs[hd][qb_] = MFMA16(ones, hd ? pb[2 * m_ + qb_] : pa[2 * m_ + qb_], m_ == 0 ? zero4 : rs[hd][qb_]); } \
            if ((u) == 1 && x < 4) { il[x >> 1][x & 1] = __builtin_amdgcn_rcpf(rs[x >> 1][x & 1][0]); OPAQUE(il[x >> 1][x & 1]); } \
            if (n + VR - 1 < 128 && qb_ == 1) { if (hd == 0) VRL(n + VR - 1); else VRH(n + VR - 1); } \
            if ((u) > 0) EPI((u) - 1, x); \
            if ((u) > 1 && ((u) & 1)) EPB((u) - 2, x); \
            SBAR(); } } while (0)
#define PVU(u, E, MID) do { PVH(u, 0, E, MID); PVH(u, 16, E, MID); PVH(u, 32, E, MID); PVH(u, 48, E, MID); } while (0)
    VRL(0); VRH(0); VRL(1); VRH(1); VRL(2); VRH(2); VRL(3); VRH(3); SBAR();
    PVU(0, -1, ); PVU(1, -1, );
    STAMP(7);
    PVU(2, -1, ); PVU(3, 46, WB(7));
    STAMP(8);
    PVU(4, -1, ); PVU(5, 0, PRIO(0));
    STAMP(9);
    PVU(6, -1, ); PVU(7, -1, );
    STAMP(10);
#pragma unroll
    for (int x = 0; x < 32; ++x) EPI(7, x);
#pragma unroll
    for (int x = 32; x < 64; ++x) EPI(7, x);
    OST(7, 1, 0); OST(7, 1, 1); OST(7, 1, 2); OST(7, 1, 3);
    STAMP(11);
}

extern "C" void kernel_launch(void* const* d_in, const int* in_sizes, int n_in, void* d_out, int out_size, void* d_ws, size_t ws_size, hipStream_t stream) {
    (void)hipFuncSetAttribute((const void*)gqa_kernel, hipFuncAttributeMaxDynamicSharedMemorySize, LDS_BYTES);
    gqa_kernel<<<256, 512, LDS_BYTES, stream>>>((const char*)d_in[0], (const char*)d_in[1], (const char*)d_in[2], (char*)d_out
   );
}
```

```cpp
#define TCH 99
#include <hip/hip_runtime.h>
#include <hip/hip_bf16.h>

typedef short bf16x8 __attribute__((ext_vector_type(8)));
typedef short s16x4 __attribute__((ext_vector_type(4)));
typedef unsigned short u16x8 __attribute__((ext_vector_type(8)));
typedef float f32x16 __attribute__((ext_vector_type(16)));
typedef float f32x4 __attribute__((ext_vector_type(4)));
typedef float f32x2 __attribute__((ext_vector_type(2)));
typedef __bf16 bf16x2 __attribute__((ext_vector_type(2)));
typedef unsigned u32x4 __attribute__((ext_vector_type(4)));
typedef unsigned u32x2 __attribute__((ext_vector_type(2)));
typedef __attribute__((address_space(3))) char* lds_ptr;
#define DI __device__ __forceinline__
#define SBAR() __builtin_amdgcn_sched_barrier(0)
#define MFMA32(a, b, c) __builtin_amdgcn_mfma_f32_32x32x16_bf16(a, b, c, 0, 0, 0)
#define MFMA16(a, b, c) __builtin_amdgcn_mfma_f32_16x16x32_bf16(a, b, c, 0, 0, 0)
#define OPAQUE(x) asm volatile("" : "+v"(x))
#define MX3(a, b, c) __builtin_fmaxf(__builtin_fmaxf((a), (b)), (c))
#define BAR() do { SBAR(); asm volatile("s_barrier" ::: "memory"); SBAR(); } while (0)
#define WB(N) do { SBAR(); asm volatile("s_waitcnt vmcnt(" #N ")\n\ts_barrier" ::: "memory"); SBAR(); } while (0)
#define PAD(n) do { if ((n) > 0) asm volatile("s_nop %0" :: "i"((n) - 1)); } while (0)
#define PRIO(n) __builtin_amdgcn_s_setprio(n)
#define STAMP(i) do { if ((i) == TCH) { SBAR(); asm volatile("s_memrealtime %0\n\ts_waitcnt lgkmcnt(0)" : "=s"(tm) :: "memory"); SBAR(); } } while (0)
#define STARG , unsigned long long& tm
#define STPASS , tm
#if defined(__HIP_DEVICE_COMPILE__)
#define NOPK __attribute__((target("no-packed-fp32-ops")))
#else
#define NOPK
#endif
#define CODE_BYTES 41700u
#define YPV 1

constexpr int LDS_BYTES = 163840;
constexpr float CS = 0.0625f * 1.4426950408889634f;
constexpr int PA1 = 10, PY0 = 16, PY1 = 8, PA2 = 8, PA3 = 7, PY2 = 16, PB1 = 5, PB2 = 6;
constexpr int VR = 5;

DI void glds16(const char* sbase, unsigned voff, unsigned lds_dst) {
    unsigned sv;
    asm volatile("s_mov_b32 %0, m0\n\ts_mov_b32 m0, %3\n\ts_nop 0\n\tglobal_load_lds_dwordx4 %1, %2 nt\n\ts_mov_b32 m0, %0"
                 : "=&s"(sv) : "v"(voff), "s"(sbase), "s"(lds_dst) : "memory");
}
template <int OFF> DI bf16x8 gload16(const char* sbase, unsigned voff) {
    bf16x8 r;
    asm volatile("global_load_dwordx4 %0, %1, %2 offset:%3 nt" : "=v"(r) : "v"(voff), "s"(sbase), "i"(OFF) : "memory");
    return r;
}
DI void gstore16(char* sbase, unsigned voff, u32x4 d) { asm volatile("global_store_dwordx4 %0, %1, %2 sc1\n\ts_nop 1" :: "v"(voff), "v"(d), "s"(sbase) : "memory"); }
DI void swap16(bf16x8& a, bf16x8& b) {
    u32x4 x = __builtin_bit_cast(u32x4, a), y = __builtin_bit_cast(u32x4, b);
#pragma unroll
    for (int j = 0; j < 4; ++j) { auto r = __builtin_amdgcn_permlane16_swap(x[j], y[j], false, false); x[j] = r[0]; y[j] = r[1]; }
    a = __builtin_bit_cast(bf16x8, x); b = __builtin_bit_cast(bf16x8, y);
}
DI void swap8(bf16x8& a, bf16x8& b) {
    u32x4 x = __builtin_bit_cast(u32x4, a), y = __builtin_bit_cast(u32x4, b);
#pragma unroll
    for (int j = 0; j < 4; ++j) { const int xj = x[j], yj = y[j];
        x[j] = __builtin_amdgcn_update_dpp(xj, yj, 0x128, 0xf, 0xc, false); y[j] = __builtin_amdgcn_update_dpp(yj, xj, 0x128, 0xf, 0x3, false); }
    a = __builtin_bit_cast(bf16x8, x); b = __builtin_bit_cast(bf16x8, y);
}
DI unsigned cvtpk(float lo, float hi) { f32x2 v = {lo, hi}; bf16x2 b = __builtin_convertvector(v, bf16x2); return __builtin_bit_cast(unsigned, b); }
DI s16x4 vtr(unsigned p) { return __builtin_bit_cast(s16x4, __builtin_amdgcn_ds_read_tr16_b64_v4i16((__attribute__((address_space(3))) s16x4*)(uintptr_t)p)); }
DI bf16x8 ldsr(lds_ptr p) { return *(const __attribute__((address_space(3))) bf16x8*)p; }
DI int lane_id() { int l; asm volatile("v_mbcnt_lo_u32_b32 %0, -1, 0\n\tv_mbcnt_hi_u32_b32 %0, -1, %0" : "=v"(l)); return l; }

DI float row_ref(const f32x16 (&s)[4]) {
    float a = s[0][0], b = s[0][1];
#pragma unroll
    for (int kb = 0; kb < 4; ++kb)
#pragma unroll
        for (int r = 0; r < 16; r += 4) { a = MX3(a, s[kb][r], s[kb][r + 1]); b = MX3(b, s[kb][r + 2], s[kb][r + 3]); }
    const unsigned m = __float_as_uint(__builtin_fmaxf(a, b));
    auto rr = __builtin_amdgcn_permlane32_swap(m, m, false, false);
    return __builtin_ceilf(__builtin_fmaxf(__uint_as_float(rr[0]), __uint_as_float(rr[1])) * CS);
}
template <int P0, typename F> DI void exp_pack(const f32x16 (&s)[4], bf16x8 (&p)[16], float ref, F hook) {
    const float nm = -ref;
#pragma unroll
    for (int t = 0; t < 8; ++t) {
        SBAR(); hook(t); SBAR();
        u32x4 w;
#pragma unroll
        for (int j = 0; j < 4; ++j)
            w[j] = cvtpk(__builtin_amdgcn_exp2f(__builtin_fmaf(s[t >> 1][8 * (t & 1) + 2 * j], CS, nm)), __builtin_amdgcn_exp2f(__builtin_fmaf(s[t >> 1][8 * (t & 1) + 2 * j + 1], CS, nm)));
        p[P0 + t] = __builtin_bit_cast(bf16x8, w); OPAQUE(p[P0 + t]);
    }
}
DI void fixup(bf16x8 (&p)[16], float r1, float r2) {
    const unsigned di = (unsigned)__builtin_fminf(r2 - r1, 255.f) * 0x00800080u; const u32x4 d4 = {di, di, di, di};
#pragma unroll
    for (int i = 0; i < 8; ++i) { p[i] = __builtin_bit_cast(bf16x8, __builtin_elementwise_sub_sat(__builtin_bit_cast(u16x8, p[i]), __builtin_bit_cast(u16x8, d4))); OPAQUE(p[i]); }
#pragma unroll
    for (int i = 0; i < 8; ++i) { swap16(p[2 * i], p[2 * i + 1]); OPAQUE(p[2 * i]); OPAQUE(p[2 * i + 1]); }
    SBAR();
}

template <bool Y> DI void scores(const char* Qa, const char* Kw, const char* Vw, const int w4, const int rot, char* lds_raw, bf16x8 (&pa)[16], bf16x8 (&pb)[16] STARG) {
    const lds_ptr lds = (lds_ptr)lds_raw;
    const unsigned ldsw = (unsigned)(uintptr_t)lds_raw + w4 * 1024;
    const int lane = lane_id(), r32 = lane & 31, hi = lane >> 5;
    const unsigned qoff = (lane & 7) * 512 + hi * 16 + ((lane >> 3) & 3) * 32;
    const unsigned koff = (lane >> 3) * 512 + (((lane & 7) ^ ((4 * w4 + (lane >> 4)) & 7)) << 4);
    bf16x8 qa[16], qb[16], kf[4];
    f32x16 s[4];
    const f32x16 zero = {};
#define LQA(i) qa[i] = gload16<((i) >> 2) * 128>(Qa + ((i) & 3) * 4096, qoff)
#define LQA4(g) do { LQA(4 * (g)); LQA(4 * (g) + 1); LQA(4 * (g) + 2); LQA(4 * (g) + 3); } while (0)
#define QWAITA(o, N) do { asm volatile("s_waitcnt vmcnt(" #N ")" : "+v"(qa[o]), "+v"(qa[o + 1]), "+v"(qa[o + 2]), "+v"(qa[o + 3]) :: "memory"); \
        swap16(qa[o], qa[o + 2]); swap16(qa[o + 1], qa[o + 3]); swap8(qa[o], qa[o + 1]); swap8(qa[o + 2], qa[o + 3]); SBAR(); } while (0)
#define QBI(k) (((k) & ~3) | (((k) & 1) << 1) | (((k) >> 1) & 1))
#define LQB(k) qb[QBI(k)] = gload16<(QBI(k) >> 1) * 64>(Qa + 131072 + (QBI(k) & 1) * 8192, qoffb)
#define LQB2(k) do { LQB(k); LQB((k) + 1); } while (0)
#define QWAITB(o, N) do { asm volatile("s_waitcnt vmcnt(" #N ")" : "+v"(qb[o]), "+v"(qb[o + 1]), "+v"(qb[o + 2]), "+v"(qb[o + 3]) :: "memory"); swap16(qb[o], qb[o + 1]); swap16(qb[o + 2], qb[o + 3]); SBAR(); } while (0)
#define LK(h, g, j) glds16(Kw + (h) * 65536 + (j) * 16384 + (g) * 128, koff, ldsw + (h) * 65536 + (g) * 16384 + (j) * 4096)
#define LK4(h, g) do { LK(h, g, 0); LK(h, g, 1); LK(h, g, 2); LK(h, g, 3); } while (0)
#define LV(sl, j, dst) glds16(Vw + (j) * 16384 + (((sl) + rot) & 3) * 128, voff, ldsw + (dst) + (j) * 4096)
#define VOFF() const int lv_ = lane_id(); const unsigned voff = (lv_ >> 3) * 512 + (((lv_ & 7) ^ (((lv_ >> 4) & 1) << 1) ^ ((w4 >> 1) << 2)) << 4)
    if (!Y) { LQA4(0); LQA4(1); } else { LK4(0, 0); LK4(0, 1); }
    lds_ptr kp[4];
#pragma unroll
    for (int j = 0; j < 4; ++j) kp[j] = lds + r32 * 128 + (((2 * j) ^ hi ^ ((r32 >> 1) & 7)) << 4);
#define KADV(d) do { _Pragma("unroll") for (int j = 0; j < 4; ++j) { kp[j] += (d); OPAQUE(kp[j]); } } while (0)
#define KF(i) kf[(i) & 3] = ldsr(kp[((i) >> 2) & 3] + ((i) >> 4) * 16384 + ((i) & 3) * 4096)
#define S_PRE(g) do { KF(16 * (g)); KF(16 * (g) + 1); KF(16 * (g) + 2); SBAR(); } while (0)
#define S_GRP(q, g, CONT, HOOK) do { \
        _Pragma("unroll") for (int ii = 0; ii < 16; ++ii) { const int i = 16 * (g) + ii; \
            if (ii + 3 < 16 || (CONT && i + 3 < 64)) KF(i + 3); \
            s[i & 3] = MFMA32(kf[i & 3], q[i >> 2], i < 4 ? zero : s[i & 3]); \
            HOOK; SBAR(); } } while (0)
    if (!Y) {
        QWAITA(0, 4);  BAR(); STAMP(1); S_PRE(0); S_GRP(qa, 0, false, PAD(PA1); if (ii == 0) LQA(8); if (ii == 4) LQA(9); if (ii == 8) LQA(10); if (ii == 12) LQA(11));
        QWAITA(4, 4);  BAR(); S_PRE(1); S_GRP(qa, 1, false, PAD(PA1); if (ii == 0) LQA(12); if (ii == 4) LQA(13); if (ii == 8) LQA(14); if (ii == 12) LQA(15));
        QWAITA(8, 4);  BAR(); S_PRE(2); S_GRP(qa, 2, false, PAD(PA1));
        QWAITA(12, 0); BAR(); S_PRE(3); S_GRP(qa, 3, true, PAD(PA1));
    } else {
        WB(4); LK4(0, 2); LK(0, 3, 0); LK(0, 3, 1); WB(6); LK(0, 3, 2); LK(0, 3, 3); LQA4(0); WB(8); LQA4(1); LQA4(2); WB(12);
        PRIO(1); S_PRE(0);
        QWAITA(0, 8); STAMP(1); S_GRP(qa, 0, true, PAD(PY0); if (ii == 0) LQA(12); if (ii == 3) LQA(13); if (ii == 6) LQA(14); if (ii == 9) LQA(15));
        PRIO(0);
        QWAITA(4, 8); S_GRP(qa, 1, true, PAD(PY1)); QWAITA(8, 4); S_GRP(qa, 2, true, PAD(PY1)); QWAITA(12, 0); S_GRP(qa, 3, true, PAD(PY1));
        BAR();
    }
    STAMP(2);
    const int lb_ = lane_id(); const unsigned qoffb = (lb_ & 15) * 512 + ((lb_ >> 4) & 1) * 32 + (lb_ >> 5) * 16;
    float r1 = row_ref(s);
    if (!Y) { exp_pack<0>(s, pa, r1, [&](int t) { LK(1, t >> 2, t & 3); }); LK4(1, 2); LK4(1, 3); LQB2(0); WB(10); }
    else    exp_pack<0>(s, pa, r1, [&](int t) { if (t == 4) BAR(); });
    OPAQUE(r1);
    KADV(65536); S_PRE(0);
    if (!Y) {
        S_GRP(qa, 0, true, PAD(PA2); if (ii == 8) LQB2(2)); S_GRP(qa, 1, false, PAD(PA2); if (ii == 8) LQB2(4));
        WB(6); S_PRE(2);
        S_GRP(qa, 2, true, PAD(PA3); if (ii == 8) LQB2(6)); S_GRP(qa, 3, true, PAD(PA3); if (ii == 8) LQB2(8));
    } else {
        S_GRP(qa, 0, true, PAD(PY2)); S_GRP(qa, 1, true, ); S_GRP(qa, 2, true, ); S_GRP(qa, 3, true, );
        LQB2(0);
    }
    {   const float r2 = __builtin_fmaxf(r1, row_ref(s));
        if (!Y) exp_pack<8>(s, pa, r2, [&](int t) { if (t == 0) LQB2(10); if (t == 3) LQB2(12); if (t == 6) LQB2(14); });
        else    exp_pack<8>(s, pa, r2, [&](int t) { if (t == 0) LQB2(2); if (t == 2) LQB2(4); if (t == 4) LQB2(6); if (t == 5) LQB2(8); if (t == 7) LQB2(10); });
        fixup(pa, r1, r2); }
    STAMP(3);
    KADV(-65536); S_PRE(0);
    if (!Y) { QWAITB(0, 12); S_GRP(qb, 0, true, PAD(PB1)); }
    else { PRIO(1); QWAITB(0, 8); S_GRP(qb, 0, true, PAD(PB1); if (ii == 2) LQB2(12); if (ii == 10) LQB2(14)); }
    QWAITB(4, 8); S_GRP(qb, 1, true, PAD(PB1)); QWAITB(8, 4); S_GRP(qb, 2, true, PAD(PB1)); QWAITB(12, 0); S_GRP(qb, 3, true, PAD(PB1));
    if (Y) PRIO(0);
    STAMP(4);
    r1 = row_ref(s);
    if (!Y) { VOFF(); exp_pack<0>(s, pb, r1, [&](int t) { LV(0, t, 131072); }); }
    else exp_pack<0>(s, pb, r1, [&](int t) { if (t == 3) BAR(); });
    OPAQUE(r1);
    KADV(65536); S_PRE(0);
    if (Y) PRIO(1);
    S_GRP(qb, 0, true, PAD(Y ? PB2 : 0)); S_GRP(qb, 1, true, PAD(Y ? PB2 : 0)); S_GRP(qb, 2, true, ); S_GRP(qb, 3, true, );
    if (Y) PRIO(0);
    STAMP(5);
    BAR();
    {   VOFF();
        if (!Y) { _Pragma("unroll") for (int j = 0; j < 8; ++j) LV(1, j, 0); }
        const float r2 = __builtin_fmaxf(r1, row_ref(s));
        if (!Y) exp_pack<8>(s, pb, r2, [&](int t) { LV(2, t, 32768); });
        else    exp_pack<8>(s, pb, r2, [&](int t) { LV(3, t, 65536); });
        fixup(pb, r1, r2); }
    if (!Y) WB(8); else PRIO(YPV);
    STAMP(6);
}

__global__ __launch_bounds__(512, 1) NOPK void gqa_kernel(const char* __restrict__ Q, const char* __restrict__ K, const char* __restrict__ V, char* __restrict__ O
) {
    unsigned long long tstart, tm; asm volatile("s_memrealtime %0\n\ts_waitcnt lgkmcnt(0)" : "=s"(tstart) :: "memory"); tm = tstart; SBAR();
    extern __shared__ __attribute__((aligned(16))) char lds_raw[];
    const unsigned lds0 = (unsigned)(uintptr_t)lds_raw;
    const int wid = __builtin_amdgcn_readfirstlane(threadIdx.x >> 6), w4 = wid & 3;
    {
        const char* pc; asm volatile("s_getpc_b64 %0" : "=s"(pc)); unsigned sv;
        asm volatile("s_mov_b32 %0, m0\n\ts_mov_b32 m0, %3\n\ts_nop 0\n\tglobal_load_lds_dword %1, %2\n\ts_mov_b32 m0, %0"
                     : "=&s"(sv) : "v"(min(threadIdx.x * 128u, CODE_BYTES)), "s"(pc), "s"(lds0 + 131072 + wid * 256) : "memory"); }
    const size_t wg = blockIdx.x;
    const int rot = (blockIdx.x >> 3) & 3;
    const char* Qa = Q + wg * 262144 + wid * 16384;
    char* Oa = O + wg * 262144 + wid * 16384;
    asm volatile("" : "+s"(Oa));
    bf16x8 pa[16], pb[16];
    if (wid < 4) scores<false>(Qa, K + wg * 131072 + w4 * 4096, V + wg * 131072 + w4 * 4096, w4, rot, lds_raw, pa, pb STPASS);
    else         scores<true>(Qa, K + wg * 131072 + w4 * 4096, V + wg * 131072 + w4 * 4096, w4, rot, lds_raw, pa, pb STPASS);

    const int ln = lane_id(), g4 = ln >> 4, q4 = (ln >> 2) & 3, p4 = ln & 3;
    unsigned vp[2];
#pragma unroll
    for (int b = 0; b < 2; ++b) { vp[b] = lds0 + 131072 + (16 * (g4 & 1) + 4 * (g4 >> 1) + q4) * 128 + ((b ^ (q4 >> 1) ^ ((g4 & 1) << 1)) << 5) + 8 * p4; OPAQUE(vp[b]); }
    lds_ptr stw = (lds_ptr)lds_raw + 98304 + wid * 4096 + (ln & 15) * 128 + (((g4 >> 1) ^ (ln & 1)) << 4) + 8 * (g4 & 1);
    const int sx = ((ln >> 1) & 3) << 5;
    lds_ptr str = (lds_ptr)lds_raw + 98304 + wid * 4096 + (ln >> 3) * 128 + (((ln & 7) ^ (ln >> 3)) << 4);
    OPAQUE(stw); OPAQUE(str);
    const unsigned ooff = (ln >> 3) * 512 + (ln & 7) * 16;
    s16x4 vl[VR], vh[VR];
    f32x4 oc[2][2][2][2], rs[2][2];
    const f32x4 zero4 = {};
    u32x4 w_[4], hold[2] = {}; u32x2 wq = {};
    float il[2][2], m0_ = 0.f, m1_ = 0.f;
    bf16x8 ones = {0x3F80, 0x3F80, 0x3F80, 0x3F80, 0x3F80, 0x3F80, 0x3F80, 0x3F80}; OPAQUE(ones);
#define VMOVE(n) do { if (((n) & 15) < 2 && (n) >= 16) { vp[(n) & 1] ^= 64; if ((((n) >> 4) & 1) == 0) vp[(n) & 1] += ((n) < 64) ? -131072 : 32768; OPAQUE(vp[(n) & 1]); } } while (0)
#define VRL(n) do { VMOVE(n); vl[(n) % VR] = vtr(vp[(n) & 1] + (((n) >> 1) & 7) * 4096); } while (0)
#define VRH(n) vh[(n) % VR] = vtr(vp[(n) & 1] + (((n) >> 1) & 7) * 4096 + 1024)
#define STW(c2, qb, x0, x1) *(__attribute__((address_space(3))) u32x2*)(stw + (((c2) << 5) ^ sx) + 2048 * (qb)) = u32x2{x0, x1}
#define STR(i) w_[i] = *(const __attribute__((address_space(3))) u32x4*)(str + (i) * 1024)
#define OST(u, hd, i) gstore16(Oa + (hd) * 131072 + ((((u) >> 1) + rot) & 3) * 128 + (i) * 4096, ooff, w_[i])
#define OEL(u, t, k) (oc[(u) & 1][(t) >> 3][((t) >> 2) & 1][((t) >> 1) & 1][2 * ((t) & 1) + (k)] * il[(t) >> 3][((t) >> 2) & 1])
#define EPI(u, x) do { const int pb_ = (u) & 1; \
        if ((x) >= 3 && (x) < 51) { const int t_ = ((x) - 3) / 3, k_ = ((x) - 3) % 3; \
            if (k_ == 0) { m0_ = OEL(u, t_, 0); OPAQUE(m0_); } if (k_ == 1) { m1_ = OEL(u, t_, 1); OPAQUE(m1_); } \
            if (k_ == 2) { const unsigned d_ = cvtpk(m0_, m1_); \
                if (t_ < 8 || pb_ == 1) { wq[t_ & 1] = d_; OPAQUE(wq); if (t_ & 1) STW(2 * pb_ + ((t_ >> 1) & 1), (t_ >> 2) & 1, wq[0], wq[1]); } \
                else { hold[(t_ >> 2) & 1][t_ & 3] = d_; OPAQUE(hold[(t_ >> 2) & 1]); } } } \
        if (pb_ == 1) { \
            if ((x) == 27) STR(0); if ((x) == 28) STR(1); if ((x) == 30) STR(2); if ((x) == 31) STR(3); \
            if ((x) == 33) STW(0, 0, hold[0][0], hold[0][1]); if ((x) == 36) STW(1, 0, hold[0][2], hold[0][3]); if ((x) == 39) STW(0, 1, hold[1][0], hold[1][1]); if ((x) == 42) STW(1, 1, hold[1][2], hold[1][3]); \
            if ((x) == 40) OST(u, 0, 0); if ((x) == 46) OST(u, 0, 1); if ((x) == 52) OST(u, 0, 2); if ((x) == 56) OST(u, 0, 3); \
            if ((x) == 54) STR(0); if ((x) == 58) STR(1); if ((x) == 60) STR(2); if ((x) == 62) STR(3); } } while (0)
#define EPB(u, x) do { if ((x) == 1) OST(u, 1, 0); if ((x) == 16) OST(u, 1, 1); if ((x) == 34) OST(u, 1, 2); if ((x) == 54) OST(u, 1, 3); } while (0)
#define PVH(u, X0, E, MID) do { \
        _Pragma("unroll") for (int x = (X0); x < (X0) + 16; ++x) { const int qb_ = x & 1, hd = (x >> 1) & 1, b_ = (x >> 2) & 1, m_ = x >> 3, n = 16 * (u) + (x >> 2); \
            if (x == (E)) { MID; } \
            const bf16x8 v_ = __builtin_shufflevector(vl[n % VR], vh[n % VR], 0, 1, 2, 3, 4, 5, 6, 7); \
            oc[(u) & 1][hd][qb_][b_] = MFMA16(v_, hd ? pb[2 * m_ + qb_] : pa[2 * m_ + qb_], m_ == 0 ? zero4 : oc[(u) & 1][hd][qb_][b_]); \
            if ((u) == 0 && b_ == 0) { SBAR(); rs[hd][qb_] = MFMA16(ones, hd ? pb[2 * m_ + qb_] : pa[2 * m_ + qb_], m_ == 0 ? zero4 : rs[hd][qb_]); } \
            if ((u) == 1 && x < 4) { il[x >> 1][x & 1] = __builtin_amdgcn_rcpf(rs[x >> 1][x & 1][0]); OPAQUE(il[x >> 1][x & 1]); } \
            if (n + VR - 1 < 128 && qb_ == 1) { if (hd == 0) VRL(n + VR - 1); else VRH(n + VR - 1); } \
            if ((u) > 0) EPI((u) - 1, x); \
            if ((u) > 1 && ((u) & 1)) EPB((u) - 2, x); \
            SBAR(); } } while (0)
#define PVU(u, E, MID) do { PVH(u, 0, E, MID); PVH(u, 16, E, MID); PVH(u, 32, E, MID); PVH(u, 48, E, MID); } while (0)
    VRL(0); VRH(0); VRL(1); VRH(1); VRL(2); VRH(2); VRL(3); VRH(3); SBAR();
    PVU(0, -1, ); PVU(1, -1, );
    STAMP(7);
    PVU(2, -1, ); PVU(3, 46, WB(7));
    STAMP(8);
    PVU(4, -1, ); PVU(5, 0, PRIO(0));
    STAMP(9);
    PVU(6, -1, ); PVU(7, -1, );
    STAMP(10);
#pragma unroll
    for (int x = 0; x < 32; ++x) EPI(7, x);
#pragma unroll
    for (int x = 32; x < 64; ++x) EPI(7, x);
    OST(7, 1, 0); OST(7, 1, 1); OST(7, 1, 2); OST(7, 1, 3);
    STAMP(11);
    asm volatile("s_waitcnt vmcnt(0)" ::: "memory");
    STAMP(12);
    while (__builtin_amdgcn_s_memrealtime() - tm < 10000ull) __builtin_amdgcn_s_sleep(2);
}

extern "C" void kernel_launch(void* const* d_in, const int* in_sizes, int n_in, void* d_out, int out_size, void* d_ws, size_t ws_size, hipStream_t stream) {
    (void)hipFuncSetAttribute((const void*)gqa_kernel, hipFuncAttributeMaxDynamicSharedMemorySize, LDS_BYTES);
    gqa_kernel<<<256, 512, LDS_BYTES, stream>>>((const char*)d_in[0], (const char*)d_in[1], (const char*)d_in[2], (char*)d_out
   );
}
```

```cpp
#include <hip/hip_runtime.h>
#include <hip/hip_bf16.h>

typedef short bf16x8 __attribute__((ext_vector_type(8)));
typedef short s16x4 __attribute__((ext_vector_type(4)));
typedef unsigned short u16x8 __attribute__((ext_vector_type(8)));
typedef float f32x4 __attribute__((ext_vector_type(4)));
typedef float f32x2 __attribute__((ext_vector_type(2)));
typedef __bf16 bf16x2 __attribute__((ext_vector_type(2)));
typedef unsigned u32x4 __attribute__((ext_vector_type(4)));
typedef unsigned u32x2 __attribute__((ext_vector_type(2)));
typedef __attribute__((address_space(3))) char* lds_ptr;
#define DI __device__ __forceinline__
#define SBAR() __builtin_amdgcn_sched_barrier(0)
#define MFMA16(a, b, c) __builtin_amdgcn_mfma_f32_16x16x32_bf16(a, b, c, 0, 0, 0)
#define OPAQUE(x) asm volatile("" : "+v"(x))
#define MX3(a, b, c) __builtin_fmaxf(__builtin_fmaxf((a), (b)), (c))
#define BAR() do { SBAR(); asm volatile("s_barrier" ::: "memory"); SBAR(); } while (0)
#define WB(N) do { SBAR(); asm volatile("s_waitcnt vmcnt(" #N ")\n\ts_barrier" ::: "memory"); SBAR(); } while (0)
#define PAD(n) do { if ((n) > 0) asm volatile("s_nop %0" :: "i"((n) - 1)); } while (0)
#define PRIO(n) __builtin_amdgcn_s_setprio(n)
#if defined(__HIP_DEVICE_COMPILE__)
#define NOPK __attribute__((target("no-packed-fp32-ops")))
#else
#define NOPK
#endif

constexpr int LDS_BYTES = 163840;
constexpr unsigned CODE_BYTES = 46200u, PFL = 32;
constexpr float CS = 0.0625f * 1.4426950408889634f;
constexpr int PA1 = 7, PY0 = 13, PY1 = 5, PA2 = 5, PA3 = 4, PY2 = 13, PS = 4, PV0 = 3;
constexpr int VR = 5;

DI void glds16(const char* sbase, unsigned voff, unsigned lds_dst) {
    unsigned sv;
    asm volatile("s_mov_b32 %0, m0\n\ts_mov_b32 m0, %3\n\ts_nop 0\n\tglobal_load_lds_dwordx4 %1, %2 nt\n\ts_mov_b32 m0, %0"
                 : "=&s"(sv) : "v"(voff), "s"(sbase), "s"(lds_dst) : "memory");
}
template <int OFF> DI bf16x8 gload16(const char* sbase, unsigned voff) {
    bf16x8 r;
    asm volatile("global_load_dwordx4 %0, %1, %2 offset:%3 nt" : "=v"(r) : "v"(voff), "s"(sbase), "i"(OFF) : "memory");
    return r;
}
DI void gstore16(char* sbase, unsigned voff, u32x4 d) { asm volatile("global_store_dwordx4 %0, %1, %2 sc1\n\ts_nop 1" :: "v"(voff), "v"(d), "s"(sbase) : "memory"); }
DI void swap8(bf16x8& a, bf16x8& b) {
    u32x4 x = __builtin_bit_cast(u32x4, a), y = __builtin_bit_cast(u32x4, b);
#pragma unroll
    for (int j = 0; j < 4; ++j) { const int xj = x[j], yj = y[j];
        x[j] = __builtin_amdgcn_update_dpp(xj, yj, 0x128, 0xf, 0xc, false); y[j] = __builtin_amdgcn_update_dpp(yj, xj, 0x128, 0xf, 0x3, false); }
    a = __builtin_bit_cast(bf16x8, x); b = __builtin_bit_cast(bf16x8, y);
}
DI unsigned cvtpk(float lo, float hi) { f32x2 v = {lo, hi}; bf16x2 b = __builtin_convertvector(v, bf16x2); return __builtin_bit_cast(unsigned, b); }
DI s16x4 vtr(unsigned p) { return __builtin_bit_cast(s16x4, __builtin_amdgcn_ds_read_tr16_b64_v4i16((__attribute__((address_space(3))) s16x4*)(uintptr_t)p)); }
DI bf16x8 ldsr(lds_ptr p) { return *(const __attribute__((address_space(3))) bf16x8*)p; }
DI int lane_id() { int l; asm volatile("v_mbcnt_lo_u32_b32 %0, -1, 0\n\tv_mbcnt_hi_u32_b32 %0, -1, %0" : "=v"(l)); return l; }

DI void row_ref(const f32x4 (&s)[8][2], float (&ref)[2], const float (&prev)[2], bool use_prev) {
    float m[2];
#pragma unroll
    for (int qb = 0; qb < 2; ++qb) { float a = s[0][qb][0], b = s[0][qb][1];
#pragma unroll
        for (int kb = 0; kb < 8; ++kb) { a = MX3(a, s[kb][qb][0], s[kb][qb][1]); b = MX3(b, s[kb][qb][2], s[kb][qb][3]); }
        m[qb] = __builtin_fmaxf(a, b); }
    auto r1 = __builtin_amdgcn_permlane16_swap(__float_as_uint(m[0]), __float_as_uint(m[1]), false, false);
    const unsigned z = __float_as_uint(__builtin_fmaxf(__uint_as_float(r1[0]), __uint_as_float(r1[1])));
    auto r2 = __builtin_amdgcn_permlane32_swap(z, z, false, false);
    const unsigned c = __float_as_uint(__builtin_ceilf(__builtin_fmaxf(__uint_as_float(r2[0]), __uint_as_float(r2[1])) * CS));
    auto r3 = __builtin_amdgcn_permlane16_swap(c, c, false, false);
#pragma unroll
    for (int qb = 0; qb < 2; ++qb) { ref[qb] = use_prev ? __builtin_fmaxf(prev[qb], __uint_as_float(r3[qb])) : __uint_as_float(r3[qb]); OPAQUE(ref[qb]); }
}
template <int P0, typename F> DI void exp_pack(const f32x4 (&s)[8][2], bf16x8 (&p)[16], const float (&ref)[2], F hook) {
#pragma unroll
    for (int t = 0; t < 8; ++t) {
        SBAR(); hook(t); SBAR();
        u32x4 w; const float nm = -ref[t & 1];
#pragma unroll
        for (int j = 0; j < 4; ++j) { const f32x4& x = s[2 * (t >> 1) + (j >> 1)][t & 1];
            w[j] = cvtpk(__builtin_amdgcn_exp2f(__builtin_fmaf(x[2 * (j & 1)], CS, nm)), __builtin_amdgcn_exp2f(__builtin_fmaf(x[2 * (j & 1) + 1], CS, nm))); }
        p[P0 + t] = __builtin_bit_cast(bf16x8, w); OPAQUE(p[P0 + t]);
    }
}
DI void fixup(bf16x8 (&p)[16], const float (&r1)[2], const float (&r2)[2]) {
#pragma unroll
    for (int qb = 0; qb < 2; ++qb) {
        const unsigned di = (unsigned)__builtin_fminf(r2[qb] - r1[qb], 255.f) * 0x00800080u; const u32x4 d4 = {di, di, di, di};
#pragma unroll
        for (int m = 0; m < 4; ++m) { p[2 * m + qb] = __builtin_bit_cast(bf16x8, __builtin_elementwise_sub_sat(__builtin_bit_cast(u16x8, p[2 * m + qb]), __builtin_bit_cast(u16x8, d4))); OPAQUE(p[2 * m + qb]); } }
    SBAR();
}

template <bool Y> DI void scores(const char* Qa, const char* Kw, const char* Vw, const int w4, const int rot, char* lds_raw, bf16x8 (&pa)[16], bf16x8 (&pb)[16], float (&ila)[2]) {
    const lds_ptr lds = (lds_ptr)lds_raw;
    const unsigned ldsw = (unsigned)(uintptr_t)lds_raw + w4 * 1024;
    const int lane = lane_id();
    const unsigned qoff = (lane & 7) * 512 + (lane >> 4) * 16 + ((lane >> 3) & 1) * 64;
    const unsigned koff = (lane >> 3) * 512 + (((lane & 7) ^ ((4 * w4 + (lane >> 4)) & 7)) << 4);
    bf16x8 qa[16], qb[16], kf[4];
    f32x4 s[8][2];
    const f32x4 zero = {};
    float r1[2], r2[2];
#define LQA(i) qa[i] = gload16<((i) >> 2) * 128>(Qa + ((i) & 3) * 4096, qoff)
#define LQA4(g) do { LQA(4 * (g)); LQA(4 * (g) + 1); LQA(4 * (g) + 2); LQA(4 * (g) + 3); } while (0)
#define QWAITA(o, N) do { asm volatile("s_waitcnt vmcnt(" #N ")" : "+v"(qa[o]), "+v"(qa[o + 1]), "+v"(qa[o + 2]), "+v"(qa[o + 3]) :: "memory"); \
        swap8(qa[o], qa[o + 1]); swap8(qa[o + 2], qa[o + 3]); SBAR(); } while (0)
#define LQB(k) qb[k] = gload16<((k) >> 2) * 128 + ((k) & 1) * 64>(Qa + 131072 + (((k) >> 1) & 1) * 8192, qoffb)
#define LQB2(k) do { LQB(k); LQB((k) + 1); } while (0)
#define QWAITB(o, N) do { asm volatile("s_waitcnt vmcnt(" #N ")" : "+v"(qb[o]), "+v"(qb[o + 1]), "+v"(qb[o + 2]), "+v"(qb[o + 3]) :: "memory"); SBAR(); } while (0)
#define LK(h, g, j) glds16(Kw + (h) * 65536 + (j) * 16384 + (g) * 128, koff, ldsw + (h) * 65536 + (g) * 16384 + (j) * 4096)
#define LK4(h, g) do { LK(h, g, 0); LK(h, g, 1); LK(h, g, 2); LK(h, g, 3); } while (0)
#define LV(sl, j, dst) glds16(Vw + (j) * 16384 + (((sl) + rot) & 3) * 128, voff, ldsw + (dst) + (j) * 4096)
#define VOFF() const int lv_ = lane_id(); const unsigned voff = (lv_ >> 3) * 512 + (((lv_ & 7) ^ ((lv_ >> 4) << 1)) << 4)
    if (!Y) { LQA4(0); LQA4(1); }
    lds_ptr kp[2];
#pragma unroll
    for (int j = 0; j < 2; ++j) kp[j] = lds + (lane & 15) * 128 + (((4 * j) ^ (lane >> 4) ^ ((lane >> 1) & 7)) << 4);
#define KADV(d) do { _Pragma("unroll") for (int j = 0; j < 2; ++j) { kp[j] += (d); OPAQUE(kp[j]); } } while (0)
#define KF(i) kf[(i) & 3] = ldsr(kp[((i) >> 3) & 1] + ((i) >> 4) * 16384 + ((i) & 7) * 2048)
#define S_PRE(g) do { KF(16 * (g)); KF(16 * (g) + 1); KF(16 * (g) + 2); SBAR(); } while (0)
#define S_GRP(q, g, CONT, HOOK) do { \
        _Pragma("unroll") for (int ii = 0; ii < 16; ++ii) { const int i = 16 * (g) + ii; \
            if (ii + 3 < 16 || (CONT && i + 3 < 64)) KF(i + 3); \
            s[i & 7][0] = MFMA16(kf[i & 3], q[4 * (g) + (ii >> 3)], i < 8 ? zero : s[i & 7][0]); SBAR(); \
            s[i & 7][1] = MFMA16(kf[i & 3], q[4 * (g) + 2 + (ii >> 3)], i < 8 ? zero : s[i & 7][1]); \
            HOOK; SBAR(); } } while (0)
    if (!Y) {
        QWAITA(0, 4);  BAR(); S_PRE(0); S_GRP(qa, 0, false, PAD(PA1); if (ii == 0) LQA(8); if (ii == 4) LQA(9); if (ii == 8) LQA(10); if (ii == 12) LQA(11));
        QWAITA(4, 4);  BAR(); S_PRE(1); S_GRP(qa, 1, false, PAD(PA1); if (ii == 0) LQA(12); if (ii == 4) LQA(13); if (ii == 8) LQA(14); if (ii == 12) LQA(15));
        QWAITA(8, 4);  BAR(); S_PRE(2); S_GRP(qa, 2, false, PAD(PA1));
        QWAITA(12, 0); BAR(); S_PRE(3); S_GRP(qa, 3, true, PAD(PA1));
    } else {
        LK4(0, 2); WB(6); LK(0, 3, 0); LK(0, 3, 1); WB(6); LK(0, 3, 2); LK(0, 3, 3); LQA4(0); WB(8); LQA4(1); LQA4(2); WB(12);
        PRIO(1); S_PRE(0);
        QWAITA(0, 8); S_GRP(qa, 0, true, PAD(PY0); if (ii == 0) LQA(12); if (ii == 3) LQA(13); if (ii == 6) LQA(14); if (ii == 9) LQA(15));
        PRIO(0);
        QWAITA(4, 8); S_GRP(qa, 1, true, PAD(PY1)); QWAITA(8, 4); S_GRP(qa, 2, true, PAD(PY1)); QWAITA(12, 0); S_GRP(qa, 3, true, PAD(PY1));
        BAR();
        PRIO(1);
    }
    const int lb_ = lane_id(); const unsigned qoffb = (lb_ & 15) * 512 + (lb_ >> 4) * 16;
    row_ref(s, r1, r1, false);
    if (!Y) { exp_pack<0>(s, pa, r1, [&](int t) { LK(1, t >> 2, t & 3); }); LK4(1, 2); LK4(1, 3); LQB2(0); WB(10); }
    else    exp_pack<0>(s, pa, r1, [&](int t) { if (t == 4) BAR(); });
    KADV(65536); S_PRE(0);
    if (!Y) {
        S_GRP(qa, 0, true, PAD(PA2); if (ii == 8) LQB2(2)); S_GRP(qa, 1, false, PAD(PA2); if (ii == 8) LQB2(4));
        WB(6); S_PRE(2);
        S_GRP(qa, 2, true, PAD(PA3); if (ii == 4) LQB2(6); if (ii == 12) LQB2(8)); S_GRP(qa, 3, true, PAD(PA3); if (ii == 2) LQB2(10); if (ii == 12) LQB2(12));
    } else {
        S_GRP(qa, 0, true, PAD(PY2)); S_GRP(qa, 1, true, PAD(PS); if (ii == 10) LQB2(0)); S_GRP(qa, 2, true, PAD(PS); if (ii == 8) LQB2(2)); S_GRP(qa, 3, true, PAD(PS); if (ii == 4) LQB2(4); if (ii == 12) LQB2(6));
        LQB2(8);
    }
    row_ref(s, r2, r1, true);
    if (!Y) exp_pack<8>(s, pa, r2, [&](int t) { if (t == 0) LQB2(14); });
    else    exp_pack<8>(s, pa, r2, [&](int t) { if (t == 1) LQB2(10); if (t == 4) LQB2(12); if (t == 7) LQB2(14); });
    fixup(pa, r1, r2);
    if (Y) PRIO(0);
    KADV(-65536); S_PRE(0);
    {   bf16x8 ones = {0x3F80, 0x3F80, 0x3F80, 0x3F80, 0x3F80, 0x3F80, 0x3F80, 0x3F80}; OPAQUE(ones); f32x4 ra[2];
#pragma unroll
        for (int i = 0; i < 16; ++i) ra[i & 1] = MFMA16(ones, pa[i], i < 2 ? zero : ra[i & 1]);
        SBAR();
        QWAITB(0, 12); S_GRP(qb, 0, true, PAD(PS); if (ii == 2 || ii == 3) { ila[ii & 1] = __builtin_amdgcn_rcpf(ra[ii & 1][0]); OPAQUE(ila[ii & 1]); }); }
    QWAITB(4, 8); S_GRP(qb, 1, true, PAD(PS)); QWAITB(8, 4); S_GRP(qb, 2, true, PAD(PS)); QWAITB(12, 0); S_GRP(qb, 3, true, PAD(PS));
    if (Y) { BAR(); PRIO(1); }
    row_ref(s, r1, r1, false);
    VOFF();
    if (!Y) exp_pack<0>(s, pb, r1, [&](int t) { if (t >= 4) LV(0, t - 4, 131072); });
    else    exp_pack<0>(s, pb, r1, [&](int t) { if (t & 1) LV(1, t >> 1, 0); });
    if (Y) PRIO(0);
    KADV(65536); S_PRE(0);
    if (!Y) { S_GRP(qb, 0, true, PAD(PS); if (ii == 0) BAR(); if ((ii & 3) == 1) LV(0, 4 + (ii >> 2), 131072)); S_GRP(qb, 1, true, PAD(PS)); }
    else    { S_GRP(qb, 0, true, PAD(PS); if ((ii & 7) == 2) LV(1, 4 + (ii >> 3), 0)); S_GRP(qb, 1, true, PAD(PS); if ((ii & 7) == 2) LV(1, 6 + (ii >> 3), 0)); }
    S_GRP(qb, 2, true, PAD(PS)); S_GRP(qb, 3, true, PAD(PS));
    if (Y) { WB(0); PRIO(0); }
    row_ref(s, r2, r1, true);
    if (!Y) exp_pack<8>(s, pb, r2, [&](int t) { LV(2, t, 32768); });
    else    exp_pack<8>(s, pb, r2, [&](int t) { LV(3, t, 65536); });
    fixup(pb, r1, r2);
    if (!Y) WB(8);
}

__global__ __launch_bounds__(512, 1) NOPK void gqa_kernel(const char* __restrict__ Q, const char* __restrict__ K, const char* __restrict__ V, char* __restrict__ O) {
    extern __shared__ __attribute__((aligned(16))) char lds_raw[];
    const unsigned lds0 = (unsigned)(uintptr_t)lds_raw;
    const int wid = __builtin_amdgcn_readfirstlane(threadIdx.x >> 6), w4 = wid & 3;
    {
        const unsigned pfo = min((((blockIdx.x >> 3) * 12u + wid * (PFL / 8) + (threadIdx.x & (PFL / 8 - 1))) % 384u) * 128u, CODE_BYTES);
        const char* pc; asm volatile("s_getpc_b64 %0" : "=s"(pc)); unsigned sv;
        asm volatile("s_mov_b32 %0, m0\n\ts_mov_b32 m0, %3\n\ts_nop 0\n\tglobal_load_lds_dword %1, %2\n\ts_mov_b32 m0, %0"
                     : "=&s"(sv) : "v"(pfo), "s"(pc), "s"(lds0 + 131072 + wid * 256) : "memory"); }
    const size_t wg = blockIdx.x;
    const int rot = (blockIdx.x >> 3) & 3;
    const char* Qa = Q + wg * 262144 + wid * 16384;
    char* Oa = O + wg * 262144 + wid * 16384;
    asm volatile("" : "+s"(Oa));
    bf16x8 pa[16], pb[16]; float il[2][2];
    {
        const int l = threadIdx.x & 63; const unsigned koff = (l >> 3) * 512 + (((l & 7) ^ ((4 * wid + (l >> 4)) & 7)) << 4);
        const char* Kh = K + wg * 131072 + wid * 4096;
#pragma unroll
        for (int i = 0; i < 4; ++i) glds16(Kh + (i & 1) * 32768 + (i >> 1) * 128, koff, lds0 + wid * 1024 + (i >> 1) * 16384 + (i & 1) * 8192); }
    if (wid >= 4) scores<true>(Qa, K + wg * 131072 + w4 * 4096, V + wg * 131072 + w4 * 4096, w4, rot, lds_raw, pa, pb, il[0]);
    else          scores<false>(Qa, K + wg * 131072 + w4 * 4096, V + wg * 131072 + w4 * 4096, w4, rot, lds_raw, pa, pb, il[0]);

    const int ln = lane_id(), g4 = ln >> 4, q4 = (ln >> 2) & 3, p4 = ln & 3;
    unsigned vp[2];
#pragma unroll
    for (int b = 0; b < 2; ++b) { vp[b] = lds0 + 131072 + (4 * g4 + q4) * 128 + ((b ^ (q4 >> 1) ^ ((g4 & 1) << 1)) << 5) + 8 * p4; OPAQUE(vp[b]); }
    lds_ptr stw = (lds_ptr)lds_raw + 98304 + wid * 4096 + (ln & 15) * 128 + (((g4 >> 1) ^ (ln & 1)) << 4) + 8 * (g4 & 1);
    const int sx = ((ln >> 1) & 3) << 5;
    lds_ptr str = (lds_ptr)lds_raw + 98304 + wid * 4096 + (ln >> 3) * 128 + (((ln & 7) ^ (ln >> 3)) << 4);
    OPAQUE(stw); OPAQUE(str);
    const unsigned ooff = (ln >> 3) * 512 + (ln & 7) * 16;
    s16x4 vl[VR], vh[VR];
    f32x4 oc[2][2][2][2], rs[2][2];
    const f32x4 zero4 = {};
    u32x4 w_[4], hold[2] = {}; u32x2 wq = {};
    float m0_ = 0.f, m1_ = 0.f;
    bf16x8 ones = {0x3F80, 0x3F80, 0x3F80, 0x3F80, 0x3F80, 0x3F80, 0x3F80, 0x3F80}; OPAQUE(ones);
#define VMOVE(n) do { if (((n) & 15) < 2 && (n) >= 16) { vp[(n) & 1] ^= 64; if ((((n) >> 4) & 1) == 0) vp[(n) & 1] += ((n) < 64) ? -131072 : 32768; OPAQUE(vp[(n) & 1]); } } while (0)
#define VRL(n) do { VMOVE(n); vl[(n) % VR] = vtr(vp[(n) & 1] + (((n) >> 1) & 7) * 4096); } while (0)
#define VRH(n) vh[(n) % VR] = vtr(vp[(n) & 1] + (((n) >> 1) & 7) * 4096 + 2048)
#define STW(c2, qb, x0, x1) *(__attribute__((address_space(3))) u32x2*)(stw + (((c2) << 5) ^ sx) + 2048 * (qb)) = u32x2{x0, x1}
#define STR(i) w_[i] = *(const __attribute__((address_space(3))) u32x4*)(str + (i) * 1024)
#define OST(u, hd, i) gstore16(Oa + (hd) * 131072 + ((((u) >> 1) + rot) & 3) * 128 + (i) * 4096, ooff, w_[i])
#define OEL(u, t, k) (oc[(u) & 1][(t) >> 3][((t) >> 2) & 1][((t) >> 1) & 1][2 * ((t) & 1) + (k)] * il[(t) >> 3][((t) >> 2) & 1])
#define EPI(u, x) do { const int pb_ = (u) & 1; \
        if ((x) >= 3 && (x) < 51) { const int t_ = ((x) - 3) / 3, k_ = ((x) - 3) % 3; \
            if (k_ == 0) { m0_ = OEL(u, t_, 0); OPAQUE(m0_); } if (k_ == 1) { m1_ = OEL(u, t_, 1); OPAQUE(m1_); } \
            if (k_ == 2) { const unsigned d_ = cvtpk(m0_, m1_); \
                if (t_ < 8 || pb_ == 1) { wq[t_ & 1] = d_; OPAQUE(wq); if (t_ & 1) STW(2 * pb_ + ((t_ >> 1) & 1), (t_ >> 2) & 1, wq[0], wq[1]); } \
                else { hold[(t_ >> 2) & 1][t_ & 3] = d_; OPAQUE(hold[(t_ >> 2) & 1]); } } } \
        if (pb_ == 1) { \
            if ((x) == 27) STR(0); if ((x) == 28) STR(1); if ((x) == 30) STR(2); if ((x) == 31) STR(3); \
            if ((x) == 33) STW(0, 0, hold[0][0], hold[0][1]); if ((x) == 36) STW(1, 0, hold[0][2], hold[0][3]); if ((x) == 39) STW(0, 1, hold[1][0], hold[1][1]); if ((x) == 42) STW(1, 1, hold[1][2], hold[1][3]); \
            if ((x) == 40) OST(u, 0, 0); if ((x) == 46) OST(u, 0, 1); if ((x) == 52) OST(u, 0, 2); if ((x) == 56) OST(u, 0, 3); \
            if ((x) == 54) STR(0); if ((x) == 58) STR(1); if ((x) == 60) STR(2); if ((x) == 62) STR(3); } } while (0)
#define EPB(u, x) do { if ((x) == 1) OST(u, 1, 0); if ((x) == 16) OST(u, 1, 1); if ((x) == 34) OST(u, 1, 2); if ((x) == 54) OST(u, 1, 3); } while (0)
#define PVH(u, X0, E, MID) do { \
        _Pragma("unroll") for (int x = (X0); x < (X0) + 16; ++x) { const int qb_ = x & 1, hd = (x >> 1) & 1, b_ = (x >> 2) & 1, m_ = x >> 3, n = 16 * (u) + (x >> 2); \
            if (x == (E)) { MID; } \
            const bf16x8 v_ = __builtin_shufflevector(vl[n % VR], vh[n % VR], 0, 1, 2, 3, 4, 5, 6, 7); \
            oc[(u) & 1][hd][qb_][b_] = MFMA16(v_, hd ? pb[2 * m_ + qb_] : pa[2 * m_ + qb_], m_ == 0 ? zero4 : oc[(u) & 1][hd][qb_][b_]); \
            if ((u) == 0 && b_ == 0 && hd == 1) { SBAR(); rs[hd][qb_] = MFMA16(ones, hd ? pb[2 * m_ + qb_] : pa[2 * m_ + qb_], m_ == 0 ? zero4 : rs[hd][qb_]); } \
            if ((u) == 1 && x >= 2 && x < 4) { il[x >> 1][x & 1] = __builtin_amdgcn_rcpf(rs[x >> 1][x & 1][0]); OPAQUE(il[x >> 1][x & 1]); } \
            if (n + VR - 1 < 128 && qb_ == 1) { if (hd == 0) VRL(n + VR - 1); else VRH(n + VR - 1); } \
            if ((u) == 0) PAD(PV0); \
            if ((u) > 0) EPI((u) - 1, x); \
            if ((u) > 1 && ((u) & 1)) EPB((u) - 2, x); \
            SBAR(); } } while (0)
#define PVU(u, E, MID) do { PVH(u, 0, E, MID); PVH(u, 16, E, MID); PVH(u, 32, E, MID); PVH(u, 48, E, MID); } while (0)
    VRL(0); VRH(0); VRL(1); VRH(1); VRL(2); VRH(2); VRL(3); VRH(3); SBAR();
    PVU(0, -1, ); PVU(1, -1, );
    PVU(2, -1, ); PVU(3, 46, WB(7));
    PVU(4, -1, ); PVU(5, -1, );
    PVU(6, -1, ); PVU(7, -1, );
#pragma unroll
    for (int x = 0; x < 32; ++x) EPI(7, x);
#pragma unroll
    for (int x = 32; x < 64; ++x) EPI(7, x);
    OST(7, 1, 0); OST(7, 1, 1); OST(7, 1, 2); OST(7, 1, 3);
}

extern "C" void kernel_launch(void* const* d_in, const int* in_sizes, int n_in, void* d_out, int out_size, void* d_ws, size_t ws_size, hipStream_t stream) {
    (void)hipFuncSetAttribute((const void*)gqa_kernel, hipFuncAttributeMaxDynamicSharedMemorySize, LDS_BYTES);
    gqa_kernel<<<256, 512, LDS_BYTES, stream>>>((const char*)d_in[0], (const char*)d_in[1], (const char*)d_in[2], (char*)d_out);
}
```
